```python
import torch

def _softmax(x, axis=-1):
    return torch.softmax(x, dim=axis)
def _gelu(x):
    return torch.nn.functional.gelu(x, approximate="tanh")
def _silu(x):
    return torch.nn.functional.silu(x)
def _sigmoid(x):
    return torch.sigmoid(x)
def _layernorm(x, axis=-1, eps=1e-5):
    m = x.mean(dim=axis, keepdim=True)
    v = x.var(dim=axis, keepdim=True, unbiased=False)
    return (x - m) / torch.sqrt(v + eps)
def _scatter_add(tgt, idx, src, axis=0):
    return tgt.clone().index_add_(axis, idx, src)
def _segment_sum(src, seg, n):
    out = torch.zeros((n,) + src.shape[1:], dtype=src.dtype)
    return out.index_add_(0, seg, src)
def _pad(x, pw):
    flat = tuple(v for pair in reversed(pw) for v in pair)
    return torch.nn.functional.pad(x, flat)

kernel_name = 'recurrent_scan__linear_kernel_hedgehog__swiglu_ffn__prompt'

def setup_inputs():

    _w = (lambda a: (lambda t: t.to(torch.bfloat16) if t.is_floating_point() else t)(torch.as_tensor(a)))
    def _A_setup():
        B, S, D, H = 1, 2048, 576, 32
        Dh = D // H
        return {
            "x": _w((lambda *s: torch.randn(*s, dtype=torch.bfloat16) * 0.05)(B, S, D)),
            "wqkv": _w((lambda *s: torch.randn(*s, dtype=torch.bfloat16) * 0.2)(D, 3 * D)),
            "w_phi": _w((lambda *s: torch.randn(*s, dtype=torch.bfloat16) * 0.2)(H, Dh, Dh) * (Dh ** -0.5)),
            "wo": _w((lambda *s: torch.randn(*s, dtype=torch.bfloat16) * 0.05)(D, D)),
        }
    _A_in = _A_setup()

    def _B_setup():
        B, S, D, F = 1, 2048, 576, 4608
        return {
            "x": _w((lambda *s: torch.randn(*s, dtype=torch.bfloat16) * 0.05)(B, S, D)),
            "w_gate": _w((lambda *s: torch.randn(*s, dtype=torch.bfloat16) * 0.05)(D, F)),
            "w_up": _w((lambda *s: torch.randn(*s, dtype=torch.bfloat16) * 0.05)(D, F)),
            "w_down": _w((lambda *s: torch.randn(*s, dtype=torch.bfloat16) * 0.05)(F, D)),
        }
    _B_in = _B_setup()

    return {
        "A_x": _A_in["x"],
        "A_wqkv": _A_in["wqkv"],
        "A_w_phi": _A_in["w_phi"],
        "A_wo": _A_in["wo"],
        "B_x": _B_in["x"],
        "B_w_gate": _B_in["w_gate"],
        "B_w_up": _B_in["w_up"],
        "B_w_down": _B_in["w_down"],
    }

def reference(A_x, A_wqkv, A_w_phi, A_wo, B_x, B_w_gate, B_w_up, B_w_down):
    def _A_ref(x, wqkv, w_phi, wo):
        B, S, D = x.shape
        H = 32; Dh = D // H
        qkv = (lambda x, *d: x.permute(*d))((x @ wqkv).reshape(B, S, 3, H, Dh), 2, 0, 3, 1, 4)
        q, k, v = qkv[0], qkv[1], qkv[2]
        pq = torch.einsum("bhsd,hdf->bhsf", q, w_phi)
        pk = torch.einsum("bhsd,hdf->bhsf", k, w_phi)
        phi_q = torch.concatenate([_softmax(pq, axis=-1), _softmax(-pq, axis=-1)], axis=-1)
        phi_k = torch.concatenate([_softmax(pk, axis=-1), _softmax(-pk, axis=-1)], axis=-1)
        kv = torch.einsum("bhsf,bhsd->bhfd", phi_k, v)
        z  = torch.einsum("bhsf->bhf", phi_k)
        num = torch.einsum("bhqf,bhfd->bhqd", phi_q, kv)
        den = torch.einsum("bhqf,bhf->bhq", phi_q, z)[..., None] + 1e-6
        o = num / den
        return (lambda x, *d: x.permute(*d))(o, 0, 2, 1, 3).reshape(B, S, D) @ wo

    def _B_ref(x, w_gate, w_up, w_down):
        g = _silu(x @ w_gate)
        u = x @ w_up
        return (g * u) @ w_down

    x = A_x
    xn = _layernorm(x, axis=-1)
    s = _A_ref(xn, A_wqkv, A_w_phi, A_wo)
    g = _silu(_B_ref(xn, B_w_gate, B_w_up, B_w_down))
    return x + s * g
```

```cpp
#include <hip/hip_runtime.h>
#include <hip/hip_bf16.h>
#include <stdint.h>

typedef short s4 __attribute__((ext_vector_type(4)));
typedef __bf16 b2 __attribute__((ext_vector_type(2)));
typedef __bf16 b8 __attribute__((ext_vector_type(8)));
typedef float f4 __attribute__((ext_vector_type(4)));
typedef float f16v __attribute__((ext_vector_type(16)));
typedef unsigned u4 __attribute__((ext_vector_type(4)));
typedef unsigned u2 __attribute__((ext_vector_type(2)));
#define LDS __attribute__((address_space(3)))
#define GLB __attribute__((address_space(1)))
#define DEV __device__ __forceinline__
typedef __amdgpu_buffer_rsrc_t rsrc_t;

#define ST_AUX 16

constexpr unsigned HP_OFF = 0, HP_SZ = 128u * 144u * 1024u;
constexpr unsigned OP_OFF = HP_OFF + HP_SZ, OP_SZ = 128u * 18u * 1024u;
constexpr unsigned WP_OFF = OP_OFF + OP_SZ, WP_SZ = 8u * 162u * 4608u;
constexpr unsigned KV_OFF = WP_OFF + WP_SZ, KV_SZ = 8u * 8u * 8u * 3072u;
constexpr unsigned FL_OFF = KV_OFF + KV_SZ;

constexpr int L_X = 32768, L_V = 98304, L_WPHI = 139264, L_KVF = 147456;

DEV unsigned pk2(float a, float b) { b2 q = {(__bf16)a, (__bf16)b}; return __builtin_bit_cast(unsigned, q); }
DEV float asf(unsigned v) { return __builtin_bit_cast(float, v); }
DEV b2 asb2(unsigned v) { return __builtin_bit_cast(b2, v); }
DEV float blo(unsigned v) { return __builtin_bit_cast(float, v << 16); }
DEV float bhi(unsigned v) { return __builtin_bit_cast(float, v & 0xffff0000u); }
DEV f16v mfma32(u4 a, u4 b, f16v c) {
  return __builtin_amdgcn_mfma_f32_32x32x16_bf16(__builtin_bit_cast(b8, a), __builtin_bit_cast(b8, b), c, 0, 0, 0);
}
DEV f4 mfma16(u4 a, u4 b, f4 c) {
  return __builtin_amdgcn_mfma_f32_16x16x32_bf16(__builtin_bit_cast(b8, a), __builtin_bit_cast(b8, b), c, 0, 0, 0);
}
DEV u2 trr(LDS char* p) { return __builtin_bit_cast(u2, __builtin_amdgcn_ds_read_tr16_b64_v4i16((LDS s4*)p)); }
DEV u4 trr2(LDS char* p, int d) { u2 a = trr(p), b = trr(p + d); return u4{a.x, a.y, b.x, b.y}; }
DEV float xadd(float v) {
  auto r = __builtin_amdgcn_permlane32_swap(__builtin_bit_cast(unsigned, v), __builtin_bit_cast(unsigned, v), false, false);
  return asf(r[0]) + asf(r[1]);
}
DEV float xmax(float v) {
  auto r = __builtin_amdgcn_permlane32_swap(__builtin_bit_cast(unsigned, v), __builtin_bit_cast(unsigned, v), false, false);
  return fmaxf(asf(r[0]), asf(r[1]));
}
DEV void dma16(unsigned ldsdst, unsigned voff, const void* sbase) {
  unsigned keep;
  asm volatile("s_mov_b32 %0, m0\n\ts_mov_b32 m0, %1\n\ts_nop 0\n\tglobal_load_lds_dwordx4 %2, %3\n\ts_mov_b32 m0, %0"
               : "=&s"(keep) : "s"(ldsdst), "v"(voff), "s"(sbase) : "memory");
}
template <int N> DEV void wait_vm() { asm volatile("s_waitcnt vmcnt(%0)" ::"i"(N) : "memory"); }
template <int N> DEV void wait_lgkm() { asm volatile("s_waitcnt lgkmcnt(%0)" ::"i"(N) : "memory"); }
DEV int lane_fresh() {
  int v;
  asm volatile("v_mbcnt_lo_u32_b32 %0, -1, 0\n\tv_mbcnt_hi_u32_b32 %0, -1, %0" : "=v"(v));
  return v;
}
DEV void bar() { asm volatile("s_barrier" ::: "memory"); }
DEV void bar_all() { asm volatile("s_waitcnt vmcnt(0) lgkmcnt(0)\n\ts_barrier" ::: "memory"); }
DEV void bar_lds() { asm volatile("s_waitcnt lgkmcnt(0)\n\ts_barrier" ::: "memory"); }
#define SB() __builtin_amdgcn_sched_barrier(0)
template <int I> struct IC { static constexpr int v = I; };
template <int B, int E, class Fn> DEV void sfor(Fn&& f) {
  if constexpr (B < E) { f(IC<B>{}); sfor<B + 1, E>(f); }
}
DEV float silu_mul(float g, float u) {
  float t = __builtin_amdgcn_exp2f(g * -1.4426950408889634f);
  return (g * __builtin_amdgcn_rcpf(1.0f + t)) * u;
}

constexpr int n_slabs(int mode) { return mode == 0 ? 27 : 18; }
constexpr int st_in(int mode, int g) {
  return mode == 0 ? ((g % 9 == 8) ? 2 : ((g % 9 == 0 && g > 0) ? 2 : 0)) : 0;
}
constexpr int n_ring(int mode) { return mode == 0 ? 6 : 4; }
constexpr int iss_at(int mode, int s) { return s < 2 ? -1 : (s < n_ring(mode) ? 0 : s - n_ring(mode) + 1); }
constexpr int vm_before(int mode, int g) {
  int s = g + 1;
  if (s >= n_slabs(mode) || s < 2) return 63;
  int n = 0;
  for (int t = s + 1; t < n_slabs(mode); ++t) if (iss_at(mode, t) <= g - 1) n += 2;
  for (int x = iss_at(mode, s); x <= g - 1; ++x) n += st_in(mode, x);
  return n;
}

template <int MODE, class Iss, class Hook>
DEV void stream(LDS char* sm, u4 (&xf)[36], f16v (&acc)[4], const unsigned (&ra)[4], Iss&& issue, Hook&& hook) {
  constexpr int NS = n_slabs(MODE);
  constexpr int NSTEP = MODE == 0 ? 27 * 16 : 9 * 16 + 9 * 12;
  u4 wf[3];
  auto rd = [&](auto NN) {
    constexpr int n = decltype(NN)::v;
    if constexpr (n < NSTEP) {
      constexpr bool p2 = (MODE == 1 && n >= 144);
      constexpr int g = p2 ? 9 + (n - 144) / 12 : n / 16;
      constexpr int i = p2 ? (n - 144) % 12 : n % 16;
      constexpr int slot = g % n_ring(MODE);
      LDS char* p = sm + ra[i / 4] + (slot >= 4 ? 32768 : 0);
      wf[n % 3] = trr2(p + (slot >= 4 ? slot - 2 : slot) * 16384 + (i % 4) * 2048, 512);
    }
  };
  rd(IC<0>{}); rd(IC<1>{});
  sfor<0, NSTEP>([&](auto NN) {
    constexpr int n = decltype(NN)::v;
    constexpr bool p2 = (MODE == 1 && n >= 144);
    constexpr int g = p2 ? 9 + (n - 144) / 12 : n / 16;
    constexpr int i = p2 ? (n - 144) % 12 : n % 16;
    constexpr int t = i / 4, sl = g % 9;
    if constexpr (i == 0) {
      if constexpr (g > 0) {
        if constexpr (vm_before(MODE, g) < 63) wait_vm<vm_before(MODE, g)>();
        wait_lgkm<4>();
        bar();
        if constexpr (g + n_ring(MODE) - 1 < NS) issue(IC<g + n_ring(MODE) - 1>{});
      }
    }
    rd(IC<n + 2>{});
    hook(IC<g>{}, IC<i>{});
    if constexpr (sl == 0 && i % 4 == 0) acc[t] = mfma32(wf[n % 3], xf[sl * 4 + i % 4], f16v{});
    else acc[t] = mfma32(wf[n % 3], xf[sl * 4 + i % 4], acc[t]);
    SB();
  });
  if constexpr (MODE == 1) asm volatile("" : "+v"(acc[0]), "+v"(acc[1]), "+v"(acc[2]));
}

DEV void softmax2(const f16v& p, unsigned (&o)[9]) {
  float mx = p[0], mn = p[0];
#pragma unroll
  for (int i = 1; i < 9; ++i) { mx = fmaxf(mx, p[i]); mn = fminf(mn, p[i]); }
  mx = xmax(mx); mn = -xmax(-mn);
  const float L = 1.4426950408889634f;
  float e[18], s1 = 0.f, s2 = 0.f, a = mx * L, b = mn * L;
#pragma unroll
  for (int i = 0; i < 9; ++i) {
    e[i] = __builtin_amdgcn_exp2f(fmaf(p[i], L, -a)); s1 += e[i];
    e[9 + i] = __builtin_amdgcn_exp2f(fmaf(p[i], -L, b)); s2 += e[9 + i];
  }
  s1 = __builtin_amdgcn_rcpf(xadd(s1)); s2 = __builtin_amdgcn_rcpf(xadd(s2));
#pragma unroll
  for (int i = 0; i < 9; ++i) { e[i] *= s1; e[9 + i] *= s2; }
#pragma unroll
  for (int i = 0; i < 9; ++i) o[i] = pk2(e[2 * i], e[2 * i + 1]);
}
DEV u4 pack8(const f16v& a, int o) {
  return u4{pk2(a[o], a[o + 1]), pk2(a[o + 2], a[o + 3]), pk2(a[o + 4], a[o + 5]), pk2(a[o + 6], a[o + 7])};
}
DEV unsigned ld_flag(const unsigned* p) { return __hip_atomic_load((GLB unsigned*)p, __ATOMIC_RELAXED, __HIP_MEMORY_SCOPE_AGENT); }
DEV void st_flag(unsigned* p, unsigned v) { __hip_atomic_store((GLB unsigned*)p, v, __ATOMIC_RELAXED, __HIP_MEMORY_SCOPE_AGENT); }

template <bool ffn>
DEV void k1_body(LDS char* sm, const char* __restrict__ x, const char* __restrict__ wqkv, const unsigned short* __restrict__ wphi,
   const char* __restrict__ wo, const char* __restrict__ wg, const char* __restrict__ wu,
   const char* __restrict__ wd, char* ws) {
  const unsigned sb = (unsigned)(uintptr_t)sm;
  const int l = threadIdx.x & 63, w = __builtin_amdgcn_readfirstlane(threadIdx.x >> 6);
  const int xcd = blockIdx.x & 7, bi = blockIdx.x >> 3;
  const int m = 4 * (xcd & 1) + (bi & 3), cc = bi >> 2;
  const int cg = 6 * (xcd >> 1) + cc;
  const int hg = 2 * (xcd >> 1) + (cc - 6);
  const int r32 = l & 31, h = l >> 5;
  const rsrc_t rws = __builtin_amdgcn_make_buffer_rsrc(ws, 0, FL_OFF, 0x00020000);

  const int drow = l >> 3, dpc = l & 7, ds0 = (drow >> 1) & 1;
  const int uG = (dpc >> 1) ^ ds0, uU = uG ^ 2;
  unsigned vG, vU;
  const char *bG, *bU;
  auto qcol = [&](int pass, int f0) {
    return pass == 0 ? (f0 < 72 ? 576 + f0 : 1152 + f0 - 72) : (f0 < 16 ? 1152 + 56 + f0 : (f0 < 88 ? f0 - 16 : 0));
  };
  unsigned nonce = 0, wp[8];
  unsigned* fl = (unsigned*)(ws + FL_OFF) + (ffn ? 0 : hg) * 128;
  if constexpr (ffn) {
    vG = (8 * w + drow) * 9216 + uG * 32 + (dpc & 1) * 16;
    vU = (8 * w + drow) * 9216 + uU * 32 + (dpc & 1) * 16;
    bG = wg + cg * 384; bU = wu + cg * 384;
  } else {
    vG = (8 * w + drow) * 3456 + 2 * qcol(0, 32 * uG + 8 * (dpc & 1));
    vU = (8 * w + drow) * 3456 + 2 * qcol(0, 32 * uU + 16 + 8 * (dpc & 1));
    bG = bU = wqkv + hg * 144;
    const int reg = (r32 & 3) + 4 * (r32 >> 3), f = 9 * ((r32 >> 2) & 1) + reg, hh = w >> 1;
#pragma unroll
    for (int j = 0; j < 8; ++j) {
      int d = 16 * (hh + (w & 1)) + 8 * (j >> 2) + 4 * h + (j & 3) - 18 * hh;
      bool ok = reg < 9 && d >= 0 && d < 18;
      unsigned v = wphi[(4 * hg + hh) * 324 + (ok ? d * 18 + f : 0)];
      wp[j] = ok ? v : 0u;
    }
    if (w == 0) {
      unsigned v = ld_flag(fl + (l < 8 ? 8 + 8 * m + l : m));
      while (__builtin_amdgcn_ballot_w64(v == nonce) != 0) ++nonce;
      if (l == 0) st_flag(fl + m, nonce);
    }
  }
  const unsigned wdst = sb + w * 1024;
  u4 xf[36];
  float s1 = 0.f, s2 = 0.f;
  {
    const unsigned xv0 = (256 * m + 32 * w + drow) * 1152 + 16 * (dpc ^ (l >> 4));
    const unsigned xv1 = (256 * m + 32 * w + 8 + drow) * 1152 + 16 * (dpc ^ (4 + (l >> 4)));
    const unsigned xd = sb + L_X + w * 16384;
    auto xiss = [&](int c) {
      dma16(xd + (c & 3) * 4096, xv0, x + c * 128);
      dma16(xd + (c & 3) * 4096 + 1024, xv1, x + c * 128);
      dma16(xd + (c & 3) * 4096 + 2048, xv0, x + c * 128 + 16 * 1152);
      dma16(xd + (c & 3) * 4096 + 3072, xv1, x + c * 128 + 16 * 1152);
    };
    xiss(0); xiss(1); xiss(2); xiss(3);
    const unsigned rs = ffn ? 64 * 9216 : 64 * 3456;
    dma16(wdst, vG, bG); dma16(wdst + 8192, vU, bU);
    dma16(wdst + 16384, vG, bG + rs); dma16(wdst + 16384 + 8192, vU, bU + rs);
    LDS char* xr = sm + L_X + w * 16384 + r32 * 128;
    const b2 one = {(__bf16)1.0f, (__bf16)1.0f};
    sfor<0, 9>([&](auto C) {
      constexpr int c = decltype(C)::v;
      wait_vm<(c < 4 ? 16 : (c < 6 ? 12 : 4 * (8 - c)))>();
#pragma unroll
      for (int s = 0; s < 4; ++s)
        xf[4 * c + s] = *(LDS u4*)(xr + (c & 3) * 4096 + 16 * ((2 * s + h) ^ ((r32 >> 1) & 7)));
      if constexpr (c + 4 < 9) { wait_lgkm<0>(); xiss(c + 4); }
#pragma unroll
      for (int s = 0; s < 4; ++s)
#pragma unroll
        for (int e = 0; e < 4; ++e) {
          b2 v = asb2(xf[4 * c + s][e]);
          s1 = __builtin_amdgcn_fdot2_f32_bf16(v, one, s1, false);
          s2 = __builtin_amdgcn_fdot2_f32_bf16(v, v, s2, false);
        }
      SB();
    });
  }
  bar_all();
  s1 = xadd(s1) * (1.0f / 576.0f);
  s2 = xadd(s2) * (1.0f / 576.0f);
  const float rstd = __builtin_amdgcn_rsqf(fmaxf(s2 - s1 * s1, 0.f) + 1e-5f), nmr = -s1 * rstd;
  auto norm1 = [&](unsigned v) { return pk2(fmaf(blo(v), rstd, nmr), fmaf(bhi(v), rstd, nmr)); };
  auto norm4 = [&](u4& v) { asm volatile("" : "+v"(v)); v = u4{norm1(v[0]), norm1(v[1]), norm1(v[2]), norm1(v[3])}; };
  unsigned ra[4];
  {
    const int g = (l >> 4) & 1, q = (l >> 2) & 3, p = l & 3;
#pragma unroll
    for (int t = 0; t < 4; ++t) ra[t] = g * 8192 + (8 * h + q) * 128 + 32 * (t ^ (2 * g + (q >> 1))) + 8 * p;
  }
  f16v acc[4];

  if constexpr (ffn) {
    auto issue = [&](auto G) {
      constexpr int g = decltype(G)::v;
      constexpr unsigned o = (g % 9) * 64u * 9216u + (g / 9) * 128u;
      dma16(wdst + (g % 6) * 16384, vG, bG + o);
      dma16(wdst + (g % 6) * 16384 + 8192, vU, bU + o);
    };
    issue(IC<2>{}); issue(IC<3>{}); issue(IC<4>{}); issue(IC<5>{});
#pragma unroll
    for (int s = 0; s < 4; ++s) norm4(xf[s]);
    const unsigned hv = HP_OFF + ((16 * m + 2 * w + (r32 >> 4)) * 144 * 64 + (r32 & 15) + 16 * h) * 16 + cg * 6144;
    unsigned hq[4];
    auto epi = [&](int tile, int pass, int e) {
      hq[e] = pk2(silu_mul(acc[tile][2 * e], acc[tile][8 + 2 * e]), silu_mul(acc[tile][2 * e + 1], acc[tile][9 + 2 * e]));
      if (e == 3)
        __builtin_amdgcn_raw_buffer_store_b128(u4{hq[0], hq[1], hq[2], hq[3]}, rws,
                                               hv + (2 * pass + (tile >> 1)) * 1024 + (tile & 1) * 512, 0, ST_AUX);
    };
    stream<0>(sm, xf, acc, ra, issue, [&](auto G, auto I) {
      constexpr int g = decltype(G)::v, i = decltype(I)::v;
      if constexpr (g < 8 && i % 4 == 1) norm4(xf[4 * (g + 1) + i / 4]);
      if constexpr (g % 9 == 8 && i >= 5) epi((i - 5) / 4, g / 9, (i - 5) % 4);
      if constexpr (g % 9 == 0 && g > 0 && i < 5) epi((i + 11) / 4, g / 9 - 1, (i + 11) % 4);
    });
    epi(2, 2, 3);
#pragma unroll
    for (int e = 0; e < 4; ++e) epi(3, 2, e);
  } else {
  {
    auto issue = [&](auto G) {
      constexpr int g = decltype(G)::v;
      if constexpr (g == 9) {
        const int l = lane_fresh(), drow = l >> 3, dpc = l & 7, uG = (dpc >> 1) ^ ((drow >> 1) & 1), uU = uG ^ 2;
        vG = (8 * w + drow) * 3456 + 2 * qcol(1, 32 * uG + 8 * (dpc & 1));
        vU = (8 * w + drow) * 3456 + 2 * qcol(1, 32 * uU + 16 + 8 * (dpc & 1));
      }
      constexpr unsigned o = (g % 9) * 64u * 3456u;
      dma16(wdst + (g % 4) * 16384, vG, bG + o);
      dma16(wdst + (g % 4) * 16384 + 8192, vU, bG + o);
    };
    *(LDS u4*)(sm + L_WPHI + w * 1024 + l * 16) =
        u4{wp[0] | wp[1] << 16, wp[2] | wp[3] << 16, wp[4] | wp[5] << 16, wp[6] | wp[7] << 16};
    if (h == 0) *(LDS u2*)(sm + L_V + (32 * w + r32) * 160 + 144) = u2{0x3f80u, 0u};
    issue(IC<2>{}); issue(IC<3>{});
#pragma unroll
    for (int s = 0; s < 4; ++s) norm4(xf[s]);
    stream<1>(sm, xf, acc, ra, issue, [&](auto G, auto I) {
      constexpr int g = decltype(G)::v, i = decltype(I)::v;
      if constexpr (g < 8 && i % 4 == 1) norm4(xf[4 * (g + 1) + i / 4]);
      if constexpr (g == 9 && i == 0) {
        const int l = lane_fresh(), h = l >> 5;
        LDS char* vrow = sm + L_V + (32 * w + (l & 31)) * 160;
#pragma unroll
        for (int a = 0; a < 4; ++a) *(LDS u4*)(sm + 65536 + (w * 4 + a) * 1024 + l * 16) = pack8(acc[a / 2], 8 * (a & 1));
        *(LDS u2*)(sm + L_KVF + w * 512 + l * 8) = u2{pk2(acc[2][0], acc[2][1]), pk2(acc[2][2], acc[2][3])};
#pragma unroll
        for (int a = 1; a < 8; ++a) {
          const f16v& c = acc[2 + a / 4];
          const int o = 4 * (a & 3);
          *(LDS u2*)(vrow + 16 * (a - 1) + 8 * h) = u2{pk2(c[o], c[o + 1]), pk2(c[o + 2], c[o + 3])};
        }
        wait_lgkm<0>();
      }
    });
  }
  const int l = lane_fresh(), r32 = l & 31, h = l >> 5;
  LDS char* vrow = sm + L_V + (32 * w + r32) * 160;
#pragma unroll
  for (int a = 0; a < 2; ++a)
    *(LDS u2*)(vrow + 112 + 16 * a + 8 * h) = u2{pk2(acc[0][4 * a], acc[0][4 * a + 1]), pk2(acc[0][4 * a + 2], acc[0][4 * a + 3])};
  u4 qf[5] = {pack8(acc[0], 8), pack8(acc[1], 0), pack8(acc[1], 8), pack8(acc[2], 0), pack8(acc[2], 8)};
  u4 kf[5];
#pragma unroll
  for (int a = 0; a < 4; ++a) kf[a] = *(LDS u4*)(sm + 65536 + (w * 4 + a) * 1024 + l * 16);
  { u2 t = *(LDS u2*)(sm + L_KVF + w * 512 + l * 8); kf[4] = u4{t.x, t.y, 0u, 0u}; }
  bar_lds();
  u4 wpf[8];
#pragma unroll
  for (int i = 0; i < 8; ++i) wpf[i] = *(LDS u4*)(sm + L_WPHI + i * 1024 + l * 16);
#pragma unroll
  for (int hh = 0; hh < 4; ++hh) {
    f16v p = mfma32(wpf[2 * hh], kf[hh], f16v{});
    p = mfma32(wpf[2 * hh + 1], kf[hh + 1], p);
    unsigned o[9];
    softmax2(p, o);
    LDS char* d = sm + (32 * w + r32) * 320 + (2 * hh + h) * 40;
#pragma unroll
    for (int i = 0; i < 4; ++i) *(LDS u2*)(d + 8 * i) = u2{o[2 * i], o[2 * i + 1]};
    *(LDS u2*)(d + 32) = u2{o[8], 0u};
  }
  bar_lds();
  {
    const int hh = w >> 1, pi = 4 * ((l >> 4) & 1) + (l & 3), tq = 8 * h + ((l >> 2) & 3);
    LDS char* pa = sm + tq * 320 + w * 40 + 8 * (pi < 4 ? pi : 4);
    LDS char* pb = sm + L_V + tq * 160 + (pi < 5 ? 2 * (18 * hh - 2 * (hh & 1)) + 8 * pi : 144);
    f16v kv = {};
#pragma unroll
    for (int s = 0; s < 16; ++s) kv = mfma32(trr2(pa + s * 5120, 1280), trr2(pb + s * 2560, 640), kv);
    if (r32 <= 20) {
#pragma unroll
      for (int a = 0; a < 3; ++a)
        __builtin_amdgcn_raw_buffer_store_b128(
            __builtin_bit_cast(u4, f4{kv[4 * a], kv[4 * a + 1], kv[4 * a + 2], kv[4 * a + 3]}), rws,
            KV_OFF + (((hg * 8 + m) * 8 + w) * 3 + a) * 1024 + l * 16, 0, 16);
    }
  }
  bar_all();
  if (w == 0 && l < 8) st_flag(fl + 8 + 8 * l + m, ld_flag(fl + l));
  const int qi = hg * 8 + m, nun = qi < 34 ? 3 : 2;
  for (int n = 0; n < nun; ++n) {
    const int ks = qi + 64 * n;
    const char* src = ks < 18 ? wo : wd;
    for (int it = w; it < 36; it += 8) {
      const int P = 64 * it + l, rho = P / 72, pc = P - 72 * rho, kq = rho >> 3, j = rho & 7;
      int row;
      if (ks < 18) {
        const int u = 4 * ks + kq, g9 = u / 9, uu = u - 9 * g9;
        const int idx = uu < 8 ? 8 * (uu & 3) + j : 32 + (j & 3), hf = uu < 8 ? uu >> 2 : j >> 2;
        const int hd = idx / 9;
        row = 18 * (4 * g9 + hd) + 9 * hf + idx - 9 * hd;
      } else {
        row = 32 * (ks - 18) + 16 * (kq >> 1) + 8 * (j >> 2) + 4 * (kq & 1) + (j & 3);
      }
      dma16(sb + n * 36864 + it * 1024, row * 1152 + pc * 16, src);
    }
  }
  u4 pq[4][3];
#pragma unroll
  for (int hh = 0; hh < 4; ++hh) {
    f16v p = mfma32(wpf[2 * hh], qf[hh], f16v{});
    p = mfma32(wpf[2 * hh + 1], qf[hh + 1], p);
    unsigned o[9];
    softmax2(p, o);
    pq[hh][0] = u4{o[0], o[1], o[2], o[3]}; pq[hh][1] = u4{o[4], o[5], o[6], o[7]}; pq[hh][2] = u4{o[8], 0u, 0u, 0u};
  }
  bar_all();
  {
    const int kq = l >> 4, q = (l >> 2) & 3, p = l & 3;
    for (int n = 0; n < nun; ++n) {
      LDS char* b = sm + n * 36864 + (8 * kq + q) * 1152 + 144 * w;
      const unsigned dst = WP_OFF + (w * 162 + qi + 64 * n) * 4608;
#pragma unroll
      for (int f = 0; f < 4; ++f)
        __builtin_amdgcn_raw_buffer_store_b128(trr2(b + 64 * (f >> 1) + 16 * p + 8 * (f & 1), 4608), rws,
                                               dst + f * 1024 + l * 16, 0, ST_AUX);
      u4 v = trr2(b + 128 + 8 * (p & 1), 4608);
      if ((l & 15) < 8) __builtin_amdgcn_raw_buffer_store_b128(v, rws, dst + 4096 + (kq * 8 + (l & 7)) * 16, 0, ST_AUX);
    }
  }
  if (w == 0) {
    const int j = l & 7;
    for (int spin = 0; spin < (1 << 22); ++spin) {
      unsigned e = ld_flag(fl + 8 + 8 * m + j);
      asm volatile("s_waitcnt vmcnt(0)" ::: "memory");
      unsigned rq = ld_flag(fl + j);
      if (l < 8) st_flag(fl + 8 + 8 * l + m, rq);
      if (__builtin_amdgcn_ballot_w64(e != nonce) == 0) break;
      __builtin_amdgcn_s_sleep(8);
    }
  }
  bar();
  {
    const int hh = w >> 1, hd = w & 1;
    const int reg = (r32 & 3) + 4 * (r32 >> 3), c = reg < 9 ? 9 * ((r32 >> 2) & 1) + reg + 2 * (hh & 1) : 20;
    const unsigned o = KV_OFF + ((hg * 64 + 2 * hh + h) * 3) * 1024 + (c + 32 * hd) * 16;
#pragma unroll
    for (int a = 0; a < 3; ++a) {
      f4 s = {};
      if (a < 2 || hd == 0) {
#pragma unroll
        for (int mm = 0; mm < 8; ++mm)
          s += __builtin_bit_cast(f4, __builtin_amdgcn_raw_buffer_load_b128(rws, o, mm * 24576 + a * 1024, 16));
      }
      *(LDS u2*)(sm + L_KVF + (hh * 3 + a) * 1024 + l * 16 + 8 * hd) = u2{pk2(s[0], s[1]), pk2(s[2], s[3])};
    }
  }
  bar_lds();
  float ov[36];
#pragma unroll
  for (int hh = 0; hh < 4; ++hh) {
    f16v o = {};
#pragma unroll
    for (int s = 0; s < 3; ++s) o = mfma32(*(LDS u4*)(sm + L_KVF + (hh * 3 + s) * 1024 + l * 16), pq[hh][s], o);
    const float rd = __builtin_amdgcn_rcpf(o[9] + 1e-6f);
#pragma unroll
    for (int i = 0; i < 9; ++i) ov[9 * hh + i] = o[i] * rd;
  }
  const unsigned ob = OP_OFF + ((16 * m + 2 * w + (r32 >> 4)) * 18 * 64 + (r32 & 15)) * 16;
#pragma unroll
  for (int i = 0; i < 4; ++i)
    __builtin_amdgcn_raw_buffer_store_b128(
        u4{pk2(ov[8 * i], ov[8 * i + 1]), pk2(ov[8 * i + 2], ov[8 * i + 3]), pk2(ov[8 * i + 4], ov[8 * i + 5]), pk2(ov[8 * i + 6], ov[8 * i + 7])},
        rws, ob + (9 * hg + 4 * h + i) * 256, 0, ST_AUX);
  __builtin_amdgcn_raw_buffer_store_b64(u2{pk2(ov[32], ov[33]), pk2(ov[34], ov[35])}, rws, ob + (9 * hg + 8) * 256 + 8 * h, 0, ST_AUX);
  }
}

__global__ void __launch_bounds__(512, 2)
k1(const char* __restrict__ x, const char* __restrict__ wqkv, const unsigned short* __restrict__ wphi,
   const char* __restrict__ wo, const char* __restrict__ wg, const char* __restrict__ wu,
   const char* __restrict__ wd, char* ws) {
  __shared__ __attribute__((aligned(1024))) char smem[163840];
  if ((blockIdx.x >> 5) < 6) k1_body<true>((LDS char*)smem, x, wqkv, wphi, wo, wg, wu, wd, ws);
  else k1_body<false>((LDS char*)smem, x, wqkv, wphi, wo, wg, wu, wd, ws);
}

#define K2_DP 6
__global__ void __launch_bounds__(256, 1)
k2(const char* __restrict__ x, const char* __restrict__ ws, char* __restrict__ out) {
  __shared__ __attribute__((aligned(1024))) char smem[122880];
  LDS char* sm = (LDS char*)smem;
  const int l = threadIdx.x & 63, w = __builtin_amdgcn_readfirstlane(threadIdx.x >> 6);
  const int xcd = blockIdx.x & 7, bi = blockIdx.x >> 3;
  const int mt = 8 * (xcd >> 1) + (bi & 7), nt = 4 * (xcd & 1) + (bi >> 3);
  const rsrc_t rws = __builtin_amdgcn_make_buffer_rsrc((void*)ws, 0, KV_OFF, 0x00020000);
  constexpr int DP = K2_DP;
  const int so = w < 2 ? 5 * w : 2 + 4 * w;
  const unsigned vl = l * 16, vh = 4096 + ((l >> 4) * 8 + (l & 7)) * 16;
  const unsigned oob = w < 2 ? 0u : 0x80000000u;
  unsigned sO[4], sH[4];
#pragma unroll
  for (int i = 0; i < 4; ++i) {
    const int tg = 4 * mt + ((w + i) & 3);
    sO[i] = OP_OFF + (tg * 18 + so) * 1024;
    sH[i] = HP_OFF + (tg * 144 + 36 * w) * 1024;
  }
  const unsigned sWo = WP_OFF + (nt * 162 + so) * 4608, sWd = WP_OFF + (nt * 162 + 18 + 36 * w) * 4608;
  u4 A[DP][5], B[DP][4];
  auto ld = [&](auto NN) {
    constexpr int n = decltype(NN)::v;
    if constexpr (n < 41) {
      const unsigned z = n == 4 ? oob : 0u;
      const unsigned wb = n < 5 ? sWo + n * 4608 : sWd + (n - 5) * 4608;
#pragma unroll
      for (int i = 0; i < 4; ++i) {
        B[n % DP][i] = __builtin_amdgcn_raw_buffer_load_b128(rws, vl | z, n < 5 ? sO[i] + n * 1024 : sH[i] + (n - 5) * 1024, 0);
        A[n % DP][i] = __builtin_amdgcn_raw_buffer_load_b128(rws, (vl | z) + i * 1024, wb, 0);
      }
      A[n % DP][4] = __builtin_amdgcn_raw_buffer_load_b128(rws, vh | z, wb, 0);
    }
  };
  sfor<0, DP - 1>(ld);
  f4 acc[4][5], own[5];
  const int tok = 64 * mt + 16 * w + (l & 15), kq = l >> 4;
  const unsigned xo = tok * 1152 + nt * 144;
  u4 xv[2]; u2 xh;
  sfor<0, 41>([&](auto NN) {
    constexpr int n = decltype(NN)::v;
    ld(IC<n + DP - 1>{});
    if constexpr (n == 36) {
      xv[0] = *(const u4*)(x + xo + 16 * kq); xv[1] = *(const u4*)(x + xo + 64 + 16 * kq);
      xh = *(const u2*)(x + xo + 128 + 8 * (kq & 1));
    }
#pragma unroll
    for (int i = 0; i < 4; ++i)
#pragma unroll
      for (int f = 0; f < 5; ++f)
        acc[i][f] = mfma16(A[n % DP][f], B[n % DP][i], (n == 0 || n == 5) ? f4{} : acc[i][f]);
    if constexpr (n == 4 || n == 40) {
#pragma unroll
      for (int i = 1; i < 4; ++i)
#pragma unroll
        for (int f = 0; f < 5; ++f)
          *(LDS f4*)(sm + (n == 4 ? 0 : 61440) + ((((w + i) & 3) * 3 + i - 1) * 5 + f) * 1024 + l * 16) = acc[i][f];
      if constexpr (n == 4) {
#pragma unroll
        for (int f = 0; f < 5; ++f) own[f] = acc[0][f];
      }
    }
    SB();
  });
  __syncthreads();
  float r[5][4];
#pragma unroll
  for (int f = 0; f < 5; ++f) {
    f4 s = own[f], g = acc[0][f];
#pragma unroll
    for (int i = 0; i < 3; ++i) {
      s += *(LDS f4*)(sm + ((w * 3 + i) * 5 + f) * 1024 + l * 16);
      g += *(LDS f4*)(sm + 61440 + ((w * 3 + i) * 5 + f) * 1024 + l * 16);
    }
#pragma unroll
    for (int e = 0; e < 4; ++e) r[f][e] = silu_mul(g[e], s[e]);
  }
#pragma unroll
  for (int P = 0; P < 2; ++P) {
    u4 o;
#pragma unroll
    for (int e = 0; e < 4; ++e) {
      const float* rr = &r[2 * P + (e >> 1)][2 * (e & 1)];
      o[e] = pk2(blo(xv[P][e]) + rr[0], bhi(xv[P][e]) + rr[1]);
    }
    *(u4*)(out + xo + 64 * P + 16 * kq) = o;
  }
  if (kq < 2)
    *(u2*)(out + xo + 128 + 8 * kq) = u2{pk2(blo(xh[0]) + r[4][0], bhi(xh[0]) + r[4][1]), pk2(blo(xh[1]) + r[4][2], bhi(xh[1]) + r[4][3])};
}

extern "C" void kernel_launch(void* const* d_in, const int* in_sizes, int n_in, void* d_out, int out_size,
                              void* d_ws, size_t ws_size, hipStream_t stream) {
  k1<<<256, 512, 0, stream>>>((const char*)d_in[0], (const char*)d_in[1], (const unsigned short*)d_in[2],
                              (const char*)d_in[3], (const char*)d_in[5], (const char*)d_in[6],
                              (const char*)d_in[7], (char*)d_ws);
  k2<<<256, 256, 0, stream>>>((const char*)d_in[0], (const char*)d_ws, (char*)d_out);
}
```

```cpp
#include <hip/hip_runtime.h>
#include <hip/hip_bf16.h>
#include <stdint.h>
#include <utility>

typedef short s4 __attribute__((ext_vector_type(4)));
typedef __bf16 b2 __attribute__((ext_vector_type(2)));
typedef __bf16 b8 __attribute__((ext_vector_type(8)));
typedef float f4 __attribute__((ext_vector_type(4)));
typedef float f16v __attribute__((ext_vector_type(16)));
typedef unsigned u4 __attribute__((ext_vector_type(4)));
typedef unsigned u2 __attribute__((ext_vector_type(2)));
#define LDS __attribute__((address_space(3)))
#define GLB __attribute__((address_space(1)))
#define DEV __device__ __forceinline__
typedef __amdgpu_buffer_rsrc_t rsrc_t;

#define ST_AUX 16

constexpr unsigned HP_OFF = 0, HP_SZ = 128u * 144u * 1024u;
constexpr unsigned OP_OFF = HP_OFF + HP_SZ, OP_SZ = 128u * 18u * 1024u;
constexpr unsigned WP_OFF = OP_OFF + OP_SZ, WP_SZ = 8u * 162u * 4608u;
constexpr unsigned KV_OFF = WP_OFF + WP_SZ, KV_SZ = 8u * 8u * 8u * 3072u;
constexpr unsigned FL_OFF = KV_OFF + KV_SZ;
constexpr unsigned F2_OFF = FL_OFF + 4096;
constexpr unsigned PX_OFF = F2_OFF + 16384, PX_SZ = 64u * 16u * 9216u;

constexpr int L_X = 32768, L_V = 98304, L_WPHI = 141312, L_KVF = 149504, VS = 168, PS = 328;

DEV unsigned pk2(float a, float b) { b2 q = {(__bf16)a, (__bf16)b}; return __builtin_bit_cast(unsigned, q); }
DEV float asf(unsigned v) { return __builtin_bit_cast(float, v); }
DEV b2 asb2(unsigned v) { return __builtin_bit_cast(b2, v); }
DEV float blo(unsigned v) { return __builtin_bit_cast(float, v << 16); }
DEV float bhi(unsigned v) { return __builtin_bit_cast(float, v & 0xffff0000u); }
DEV f16v mfma32(u4 a, u4 b, f16v c) {
  return __builtin_amdgcn_mfma_f32_32x32x16_bf16(__builtin_bit_cast(b8, a), __builtin_bit_cast(b8, b), c, 0, 0, 0);
}
DEV f4 mfma16(u4 a, u4 b, f4 c) {
  return __builtin_amdgcn_mfma_f32_16x16x32_bf16(__builtin_bit_cast(b8, a), __builtin_bit_cast(b8, b), c, 0, 0, 0);
}
DEV u2 trr(LDS char* p) { return __builtin_bit_cast(u2, __builtin_amdgcn_ds_read_tr16_b64_v4i16((LDS s4*)p)); }
DEV u4 trr2(LDS char* p, int d) { u2 a = trr(p), b = trr(p + d); return u4{a.x, a.y, b.x, b.y}; }
DEV float xadd(float v) {
  auto r = __builtin_amdgcn_permlane32_swap(__builtin_bit_cast(unsigned, v), __builtin_bit_cast(unsigned, v), false, false);
  return asf(r[0]) + asf(r[1]);
}
DEV float xmax(float v) {
  auto r = __builtin_amdgcn_permlane32_swap(__builtin_bit_cast(unsigned, v), __builtin_bit_cast(unsigned, v), false, false);
  return fmaxf(asf(r[0]), asf(r[1]));
}
DEV void dma16(unsigned ldsdst, unsigned voff, const void* sbase) {
  unsigned keep;
  asm volatile("s_mov_b32 %0, m0\n\ts_mov_b32 m0, %1\n\ts_nop 0\n\tglobal_load_lds_dwordx4 %2, %3\n\ts_mov_b32 m0, %0"
               : "=&s"(keep) : "s"(ldsdst), "v"(voff), "s"(sbase) : "memory");
}
template <int K> DEV void dma16_if(int w, unsigned ldsdst, unsigned voff, const void* sbase) {
  unsigned keep;
  asm volatile("s_cmp_lg_u32 %4, %5\n\ts_cbranch_scc1 .Lskip%=\n\ts_mov_b32 %0, m0\n\ts_mov_b32 m0, %1\n\ts_nop 0\n\t"
               "global_load_lds_dwordx4 %2, %3\n\ts_mov_b32 m0, %0\n.Lskip%=:"
               : "=&s"(keep) : "s"(ldsdst), "v"(voff), "s"(sbase), "s"(w), "i"(K) : "memory", "scc");
}
template <int N> DEV void wait_vm() { asm volatile("s_waitcnt vmcnt(%0)" ::"i"(N) : "memory"); }
template <int N> DEV void wait_lgkm() { asm volatile("s_waitcnt lgkmcnt(%0)" ::"i"(N) : "memory"); }
DEV int lane_fresh() {
  int v;
  asm volatile("v_mbcnt_lo_u32_b32 %0, -1, 0\n\tv_mbcnt_hi_u32_b32 %0, -1, %0" : "=v"(v));
  return v;
}
DEV void bar() { asm volatile("s_barrier" ::: "memory"); }
DEV void bar_all() { asm volatile("s_waitcnt vmcnt(0) lgkmcnt(0)\n\ts_barrier" ::: "memory"); }
DEV void bar_lds() { asm volatile("s_waitcnt lgkmcnt(0)\n\ts_barrier" ::: "memory"); }
#define SB() __builtin_amdgcn_sched_barrier(0)
#define STAMP_DECL
#define STAMP(i)
#define STAMP_OUT(ws, k)
template <int I> struct IC { static constexpr int v = I; };
template <int B, class Fn, int... I> DEV void sfor_(Fn&& f, std::integer_sequence<int, I...>) { (f(IC<B + I>{}), ...); }
template <int B, int E, class Fn> DEV void sfor(Fn&& f) { sfor_<B>(f, std::make_integer_sequence<int, E - B>{}); }
DEV float silu_mul(float g, float u) {
  float t = __builtin_amdgcn_exp2f(g * -1.4426950408889634f);
  return (g * __builtin_amdgcn_rcpf(1.0f + t)) * u;
}

#define PFD 3
#define SYNC0 2
#define NPRE 1
#define LOOK 6
constexpr int n_slabs(int mode) { return mode == 0 ? 27 : 18; }
constexpr int n_ring(int mode) { return mode == 0 ? 9 : 4; }
constexpr bool is_sync(int mode, int g) { return mode == 1 || g <= SYNC0 || g % 3 == 0 || g == 25; }
constexpr int next_sync(int mode, int g) { int x = g + 1; while (!is_sync(mode, x)) ++x; return x; }
constexpr int iss_at(int mode, int s) {
  const int la = mode == 0 ? LOOK : 3;
  return s < 2 ? -2 : (s < la ? -1 : s - la);
}
constexpr int w_ops(int mode, int g) { int n = 0; for (int s = 2; s < n_slabs(mode); ++s) if (iss_at(mode, s) == g) n += 2; return n; }
constexpr int ex_in(int mode, int g) {
  return mode == 0 ? ((g % 9 == 8 || (g % 9 == 0 && g > 0)) ? 2 : ((g == 21 || g == 25) ? 5 : 0)) : 0;
}
constexpr int vm_before(int mode, int g) {
  int s = next_sync(mode, g), n = 63;
  if (s >= n_slabs(mode)) s = n_slabs(mode) - 1;
  if (s >= 2 && iss_at(mode, s) < g) {
    n = 0;
    for (int t = s + 1; t < n_slabs(mode); ++t) if (iss_at(mode, t) < g) n += 2;
    for (int x = iss_at(mode, s) + 1; x < g; ++x) n += ex_in(mode, x);
  }
  if (mode == 0 && g == 25) n = 0;
  return n;
}

template <int MODE, class Iss, class Hook>
DEV void stream(LDS char* sm, u4 (&xf)[36], f16v (&acc)[4], unsigned (&ra)[4], Iss&& issue, Hook&& hook) {
  constexpr int NS = n_slabs(MODE);
  constexpr int NSTEP = MODE == 0 ? 27 * 16 : 9 * 16 + 9 * 12;
  u4 wf[PFD + 1];
  auto rd = [&](auto NN) {
    constexpr int n = decltype(NN)::v;
    if constexpr (n < NSTEP) {
      constexpr bool p2 = (MODE == 1 && n >= 144);
      constexpr int g = p2 ? 9 + (n - 144) / 12 : n / 16;
      constexpr int i = p2 ? (n - 144) % 12 : n % 16;
      if constexpr (MODE == 0 && g > 0 && g % 3 == 0 && i % 4 == 0) {
        ra[i / 4] += (g % 9 == 0) ? -98304 : 49152;
        asm volatile("" : "+v"(ra[i / 4]));
      }
      wf[n % (PFD + 1)] = trr2(sm + ra[i / 4] + (g % (MODE == 0 ? 3 : 4)) * 16384 + (i % 4) * 2048, 512);
    }
  };
  sfor<0, PFD>(rd);
  sfor<0, NS>([&](auto GG) { sfor<0, ((MODE == 1 && decltype(GG)::v >= 9) ? 12 : 16)>([&](auto II) {
    constexpr int g = decltype(GG)::v, i = decltype(II)::v;
    constexpr int n = (MODE == 1 && g >= 9) ? 144 + (g - 9) * 12 + i : g * 16 + i;
    constexpr int t = i / 4, sl = g % 9;
    if constexpr (i == 0 && g > 0 && is_sync(MODE, g)) {
      if constexpr (vm_before(MODE, g) < 63) wait_vm<vm_before(MODE, g)>();
      wait_lgkm<2 * PFD>();
      bar();
    }
    sfor<2, NS>([&](auto SS) { if constexpr (iss_at(MODE, decltype(SS)::v) == g) issue(SS, II, IC<((MODE == 1 && g >= 9) ? 4 : 8)>{}); });
    rd(IC<n + PFD>{});
    hook(GG, II);
    if constexpr (sl == 0 && i % 4 == 0) acc[t] = mfma32(wf[n % (PFD + 1)], xf[sl * 4 + i % 4], f16v{});
    else acc[t] = mfma32(wf[n % (PFD + 1)], xf[sl * 4 + i % 4], acc[t]);
    SB();
  }); });
  if constexpr (MODE == 1) asm volatile("" : "+v"(acc[0]), "+v"(acc[1]), "+v"(acc[2]));
}

DEV void softmax2(const f16v& p, unsigned (&o)[9]) {
  float mx = p[0], mn = p[0];
#pragma unroll
  for (int i = 1; i < 9; ++i) { mx = fmaxf(mx, p[i]); mn = fminf(mn, p[i]); }
  mx = xmax(mx); mn = -xmax(-mn);
  const float L = 1.4426950408889634f;
  float e[18], s1 = 0.f, s2 = 0.f, a = mx * L, b = mn * L;
#pragma unroll
  for (int i = 0; i < 9; ++i) {
    e[i] = __builtin_amdgcn_exp2f(fmaf(p[i], L, -a)); s1 += e[i];
    e[9 + i] = __builtin_amdgcn_exp2f(fmaf(p[i], -L, b)); s2 += e[9 + i];
  }
  s1 = __builtin_amdgcn_rcpf(xadd(s1)); s2 = __builtin_amdgcn_rcpf(xadd(s2));
#pragma unroll
  for (int i = 0; i < 9; ++i) { e[i] *= s1; e[9 + i] *= s2; }
#pragma unroll
  for (int i = 0; i < 9; ++i) o[i] = pk2(e[2 * i], e[2 * i + 1]);
}
DEV u4 pack8(const f16v& a, int o) {
  return u4{pk2(a[o], a[o + 1]), pk2(a[o + 2], a[o + 3]), pk2(a[o + 4], a[o + 5]), pk2(a[o + 6], a[o + 7])};
}
DEV unsigned ld_flag(const unsigned* p) { return __hip_atomic_load((GLB unsigned*)p, __ATOMIC_RELAXED, __HIP_MEMORY_SCOPE_AGENT); }
DEV void st_flag(unsigned* p, unsigned v) { __hip_atomic_store((GLB unsigned*)p, v, __ATOMIC_RELAXED, __HIP_MEMORY_SCOPE_AGENT); }

template <bool ffn>
DEV void k1_body(LDS char* sm, const char* __restrict__ x, const char* __restrict__ wqkv, const unsigned short* __restrict__ wphi,
   const char* __restrict__ wo, const char* __restrict__ wg, const char* __restrict__ wu,
   const char* __restrict__ wd, char* ws) {
  STAMP_DECL STAMP(0);
  const unsigned sb = (unsigned)(uintptr_t)sm;
  const int l = threadIdx.x & 63, w = __builtin_amdgcn_readfirstlane(threadIdx.x >> 6);
  const int xcd = blockIdx.x & 7, bi = blockIdx.x >> 3;
  const int m = 2 * (xcd & 3) + (bi & 1), cc = bi >> 1;
  const int cg = 12 * (xcd >> 2) + cc;
  const int hg = 4 * (xcd >> 2) + (cc - 12);
  const int r32 = l & 31, h = l >> 5;
  const rsrc_t rws = __builtin_amdgcn_make_buffer_rsrc(ws, 0, FL_OFF, 0x00020000);

  const int drow = l >> 3, dpc = l & 7, ds0 = (drow >> 1) & 1;
  const int uG = (dpc >> 1) ^ ds0, uU = uG ^ 2;
  unsigned vG, vU;
  const char *bG, *bU;
  auto qcol = [&](int pass, int f0) {
    return pass == 0 ? (f0 < 72 ? 576 + f0 : 1152 + f0 - 72) : (f0 < 16 ? 1152 + 56 + f0 : (f0 < 88 ? f0 - 16 : 0));
  };
  unsigned nonce = 0, wp[8];
  unsigned* fl = (unsigned*)(ws + FL_OFF) + (ffn ? 0 : hg) * 128;
  if constexpr (ffn) {
    vG = (8 * w + drow) * 9216 + uG * 32 + (dpc & 1) * 16;
    vU = (8 * w + drow) * 9216 + uU * 32 + (dpc & 1) * 16;
    bG = wg + cg * 384; bU = wu + cg * 384;
  } else {
    vG = (8 * w + drow) * 3456 + 2 * qcol(0, 32 * uG + 8 * (dpc & 1));
    vU = (8 * w + drow) * 3456 + 2 * qcol(0, 32 * uU + 16 + 8 * (dpc & 1));
    bG = bU = wqkv + hg * 144;
    const int reg = (r32 & 3) + 4 * (r32 >> 3), f = 9 * ((r32 >> 2) & 1) + reg, hh = w >> 1;
#pragma unroll
    for (int j = 0; j < 8; ++j) {
      int d = 16 * (hh + (w & 1)) + 8 * (j >> 2) + 4 * h + (j & 3) - 18 * hh;
      bool ok = reg < 9 && d >= 0 && d < 18;
      unsigned v = wphi[(4 * hg + hh) * 324 + (ok ? d * 18 + f : 0)];
      wp[j] = ok ? v : 0u;
    }
    if (w == 0) {
      unsigned v = ld_flag(fl + (l < 8 ? 8 + 8 * m + l : m));
      while (__builtin_amdgcn_ballot_w64(v == nonce) != 0) ++nonce;
      if (l == 0) st_flag(fl + m, nonce);
    }
  }
  if (threadIdx.x >= 496) st_flag((unsigned*)(ws + F2_OFF) + blockIdx.x * 16 + (l & 15), 0u);
  const unsigned wdst = sb + w * 1024;
  u4 xf[36];
  float s1 = 0.f, s2 = 0.f;
  {
    const unsigned xv0 = (256 * m + 32 * w + drow) * 1152 + 16 * (dpc ^ (l >> 4));
    const unsigned xv1 = (256 * m + 32 * w + 8 + drow) * 1152 + 16 * (dpc ^ (4 + (l >> 4)));
    const unsigned xd = sb + L_X + w * 16384;
    auto xiss = [&](int c) {
      dma16(xd + (c & 3) * 4096, xv0, x + c * 128);
      dma16(xd + (c & 3) * 4096 + 1024, xv1, x + c * 128);
      dma16(xd + (c & 3) * 4096 + 2048, xv0, x + c * 128 + 16 * 1152);
      dma16(xd + (c & 3) * 4096 + 3072, xv1, x + c * 128 + 16 * 1152);
    };
    xiss(0); xiss(1); xiss(2); xiss(3);
    const unsigned rs = ffn ? 64 * 9216 : 64 * 3456;
    dma16(wdst, vG, bG); dma16(wdst + 8192, vU, bU);
    dma16(wdst + 16384, vG, bG + rs); dma16(wdst + 16384 + 8192, vU, bU + rs);
    LDS char* xr = sm + L_X + w * 16384 + r32 * 128;
    const b2 one = {(__bf16)1.0f, (__bf16)1.0f};
    sfor<0, 9>([&](auto C) {
      constexpr int c = decltype(C)::v;
      wait_vm<(c < 4 ? 16 : (c < 6 ? 12 : 4 * (8 - c)))>();
#pragma unroll
      for (int s = 0; s < 4; ++s)
        xf[4 * c + s] = *(LDS u4*)(xr + (c & 3) * 4096 + 16 * ((2 * s + h) ^ ((r32 >> 1) & 7)));
      if constexpr (c + 4 < 9) { wait_lgkm<0>(); xiss(c + 4); }
#pragma unroll
      for (int s = 0; s < 4; ++s)
#pragma unroll
        for (int e = 0; e < 4; ++e) {
          b2 v = asb2(xf[4 * c + s][e]);
          s1 = __builtin_amdgcn_fdot2_f32_bf16(v, one, s1, false);
          s2 = __builtin_amdgcn_fdot2_f32_bf16(v, v, s2, false);
        }
      SB();
    });
  }
  STAMP(1);
  bar_all();
  STAMP(2);
  s1 = xadd(s1) * (1.0f / 576.0f);
  s2 = xadd(s2) * (1.0f / 576.0f);
  const float rstd = __builtin_amdgcn_rsqf(fmaxf(s2 - s1 * s1, 0.f) + 1e-5f), nmr = -s1 * rstd;
  auto norm1 = [&](unsigned v) { return pk2(fmaf(blo(v), rstd, nmr), fmaf(bhi(v), rstd, nmr)); };
  auto norm4 = [&](u4& v) { asm volatile("" : "+v"(v)); v = u4{norm1(v[0]), norm1(v[1]), norm1(v[2]), norm1(v[3])}; };
  unsigned ra[4];
  {
    const int g = (l >> 4) & 1, q = (l >> 2) & 3, p = l & 3;
#pragma unroll
    for (int t = 0; t < 4; ++t) ra[t] = g * 8192 + (8 * h + q) * 128 + 32 * (t ^ (2 * g + (q >> 1))) + 8 * p;
  }
  f16v acc[4];

  if constexpr (ffn) {
    auto issue = [&](auto G, auto I, auto) {
      constexpr int g = decltype(G)::v, i = decltype(I)::v;
      constexpr unsigned o = (g % 9) * 64u * 9216u + (g / 9) * 128u;
      if constexpr (i < 8) dma16_if<i>(w, wdst + (g % 9) * 16384, vG, bG + o);
      else dma16_if<i - 8>(w, wdst + (g % 9) * 16384 + 8192, vU, bU + o);
    };
#pragma unroll
    for (int s = 0; s < 4 * NPRE; ++s) {
      if (s < LOOK - 2) {
        dma16(wdst + (s + 2) * 16384, vG, bG + (s + 2) * 64u * 9216u);
        dma16(wdst + (s + 2) * 16384 + 8192, vU, bU + (s + 2) * 64u * 9216u);
      }
      norm4(xf[s]);
    }
    const unsigned hv = HP_OFF + ((16 * m + 2 * w + (r32 >> 4)) * 144 * 64 + (r32 & 15) + 16 * h) * 16 + cg * 6144;
    unsigned hq[4];
    auto epi = [&](int tile, int pass, int e) {
      hq[e] = pk2(silu_mul(acc[tile][2 * e], acc[tile][8 + 2 * e]), silu_mul(acc[tile][2 * e + 1], acc[tile][9 + 2 * e]));
      if (e == 3)
        __builtin_amdgcn_raw_buffer_store_b128(u4{hq[0], hq[1], hq[2], hq[3]}, rws,
                                               hv + (2 * pass + (tile >> 1)) * 1024 + (tile & 1) * 512, 0, ST_AUX);
    };
    const int ks = min((int)blockIdx.x, 161);
    auto rt_load = [&](int e) {
      const int it = min(w + 8 * e, 35);
      const int P = 64 * it + lane_fresh(), rho = P / 72, pc = P - 72 * rho, kq = rho >> 3, j = rho & 7;
      int row = 32 * (ks - 18) + 16 * (kq >> 1) + 8 * (j >> 2) + 4 * (kq & 1) + (j & 3);
      if (ks < 18) {
        const int u = 4 * ks + kq, g9 = u / 9, uu = u - 9 * g9;
        const int idx = uu < 8 ? 8 * (uu & 3) + j : 32 + (j & 3), hf = uu < 8 ? uu >> 2 : j >> 2;
        const int hd = idx / 9;
        row = 18 * (4 * g9 + hd) + 9 * hf + idx - 9 * hd;
      }
      dma16(sb + it * 1024, row * 1152 + pc * 16, ks < 18 ? wo : wd);
    };
    auto rt_store = [&](int f) {
      const int l = lane_fresh(), kq = l >> 4, p = l & 3;
      LDS char* b = sm + (8 * kq + ((l >> 2) & 3)) * 1152 + 144 * w;
      const unsigned dst = WP_OFF + (w * 162 + ks) * 4608;
      if (f < 4)
        __builtin_amdgcn_raw_buffer_store_b128(trr2(b + 64 * (f >> 1) + 16 * p + 8 * (f & 1), 4608), rws, dst + f * 1024 + l * 16, 0, ST_AUX);
      else
        __builtin_amdgcn_raw_buffer_store_b128(trr2(b + 128 + 8 * (p & 1), 4608), rws,
                                               (l & 8) ? 0x80000000u : dst + 4096 + (kq * 8 + (l & 7)) * 16, 0, ST_AUX);
    };
    stream<0>(sm, xf, acc, ra, issue, [&](auto G, auto I) {
      constexpr int g = decltype(G)::v, i = decltype(I)::v;
      if constexpr (g == 21 && i % 2 == 0 && i < 10) rt_load(i / 2);
      if constexpr (g == 25 && i % 2 == 0 && i < 10) rt_store(i / 2);
      if constexpr (g + NPRE < 9 && i % 4 == 1) norm4(xf[4 * (g + NPRE) + i / 4]);
      if constexpr (g % 9 == 0 && i == 0) STAMP(3 + g / 9);
      if constexpr (g % 9 == 8 && i >= 6) epi((i - 6) / 4, g / 9, (i - 6) % 4);
      if constexpr (g % 9 == 0 && g > 0 && i < 6) epi((i + 10) / 4, g / 9 - 1, (i + 10) % 4);
    });
    epi(2, 2, 2); epi(2, 2, 3);
#pragma unroll
    for (int e = 0; e < 4; ++e) epi(3, 2, e);
    STAMP(6); STAMP_OUT(ws, 0);
  } else {
  {
    auto issue = [&](auto G, auto I, auto D) {
      constexpr int g = decltype(G)::v, i = decltype(I)::v, d = decltype(D)::v;
      if constexpr (g == 9 && i == 0) {
        const int l = lane_fresh(), drow = l >> 3, dpc = l & 7, uG = (dpc >> 1) ^ ((drow >> 1) & 1), uU = uG ^ 2;
        vG = (8 * w + drow) * 3456 + 2 * qcol(1, 32 * uG + 8 * (dpc & 1));
        vU = (8 * w + drow) * 3456 + 2 * qcol(1, 32 * uU + 16 + 8 * (dpc & 1));
      }
      constexpr unsigned o = (g % 9) * 64u * 3456u;
      if constexpr (i < 8) dma16_if<i>(w, wdst + (g % 4) * 16384, vG, bG + o);
      if constexpr (i >= d) dma16_if<i - d>(w, wdst + (g % 4) * 16384 + 8192, vU, bG + o);
    };
    *(LDS u4*)(sm + L_WPHI + w * 1024 + l * 16) =
        u4{wp[0] | wp[1] << 16, wp[2] | wp[3] << 16, wp[4] | wp[5] << 16, wp[6] | wp[7] << 16};
    dma16(wdst + 32768, vG, bG + 128u * 3456u);
    dma16(wdst + 32768 + 8192, vU, bG + 128u * 3456u);
#pragma unroll
    for (int s = 0; s < 4 * NPRE; ++s) norm4(xf[s]);
    stream<1>(sm, xf, acc, ra, issue, [&](auto G, auto I) {
      constexpr int g = decltype(G)::v, i = decltype(I)::v;
      if constexpr (g + NPRE < 9 && i % 4 == 1) norm4(xf[4 * (g + NPRE) + i / 4]);
      if constexpr (g % 9 == 0 && i == 0) STAMP(3 + g / 9);
      if constexpr (g == 9 && i == 0) {
        const int l = lane_fresh(), h = l >> 5;
        LDS char* vrow = sm + L_V + (32 * w + (l & 31)) * VS;
        if (h == 0) *(LDS u2*)(vrow + 144) = u2{0x3f80u, 0u};
#pragma unroll
        for (int a = 0; a < 4; ++a) *(LDS u4*)(sm + 65536 + (w * 4 + a) * 1024 + l * 16) = pack8(acc[a / 2], 8 * (a & 1));
        *(LDS u2*)(sm + L_KVF + w * 512 + l * 8) = u2{pk2(acc[2][0], acc[2][1]), pk2(acc[2][2], acc[2][3])};
#pragma unroll
        for (int a = 1; a < 8; ++a) {
          const f16v& c = acc[2 + a / 4];
          const int o = 4 * (a & 3);
          *(LDS u2*)(vrow + 16 * (a - 1) + 8 * h) = u2{pk2(c[o], c[o + 1]), pk2(c[o + 2], c[o + 3])};
        }
        wait_lgkm<0>();
      }
    });
  }
  const int l = lane_fresh(), r32 = l & 31, h = l >> 5;
  LDS char* vrow = sm + L_V + (32 * w + r32) * VS;
#pragma unroll
  for (int a = 0; a < 2; ++a)
    *(LDS u2*)(vrow + 112 + 16 * a + 8 * h) = u2{pk2(acc[0][4 * a], acc[0][4 * a + 1]), pk2(acc[0][4 * a + 2], acc[0][4 * a + 3])};
  u4 qf[5] = {pack8(acc[0], 8), pack8(acc[1], 0), pack8(acc[1], 8), pack8(acc[2], 0), pack8(acc[2], 8)};
  u4 kf[5];
#pragma unroll
  for (int a = 0; a < 4; ++a) kf[a] = *(LDS u4*)(sm + 65536 + (w * 4 + a) * 1024 + l * 16);
  { u2 t = *(LDS u2*)(sm + L_KVF + w * 512 + l * 8); kf[4] = u4{t.x, t.y, 0u, 0u}; }
  bar_lds();
  STAMP(5);
  u4 wpf[8];
#pragma unroll
  for (int i = 0; i < 8; ++i) wpf[i] = *(LDS u4*)(sm + L_WPHI + i * 1024 + l * 16);
#pragma unroll
  for (int hh = 0; hh < 4; ++hh) {
    f16v p = mfma32(wpf[2 * hh], kf[hh], f16v{});
    p = mfma32(wpf[2 * hh + 1], kf[hh + 1], p);
    unsigned o[9];
    softmax2(p, o);
    LDS char* d = sm + (32 * w + r32) * PS + (2 * hh + h) * 40;
#pragma unroll
    for (int i = 0; i < 4; ++i) *(LDS u2*)(d + 8 * i) = u2{o[2 * i], o[2 * i + 1]};
    *(LDS u2*)(d + 32) = u2{o[8], 0u};
  }
  bar_lds();
  {
    const int hh = w >> 1, pi = 4 * ((l >> 4) & 1) + (l & 3), tq = 8 * h + ((l >> 2) & 3);
    LDS char* pa = sm + tq * PS + w * 40 + 8 * (pi < 4 ? pi : 4);
    LDS char* pb = sm + L_V + tq * VS + (pi < 5 ? 2 * (18 * hh - 2 * (hh & 1)) + 8 * pi : 144);
    f16v kv = {};
#pragma unroll
    for (int s = 0; s < 16; ++s) kv = mfma32(trr2(pa + s * 16 * PS, 4 * PS), trr2(pb + s * 16 * VS, 4 * VS), kv);
    if (r32 <= 20) {
#pragma unroll
      for (int a = 0; a < 3; ++a)
        __builtin_amdgcn_raw_buffer_store_b128(
            __builtin_bit_cast(u4, f4{kv[4 * a], kv[4 * a + 1], kv[4 * a + 2], kv[4 * a + 3]}), rws,
            KV_OFF + (((hg * 8 + m) * 8 + w) * 3 + a) * 1024 + l * 16, 0, 16);
    }
  }
  unsigned rq = 0;
  if (w == 0) rq = ld_flag(fl + (l & 7));
  u4 pq[4][3];
  auto phi_q = [&](int hh) {
    f16v p = mfma32(wpf[2 * hh], qf[hh], f16v{});
    p = mfma32(wpf[2 * hh + 1], qf[hh + 1], p);
    unsigned o[9];
    softmax2(p, o);
    pq[hh][0] = u4{o[0], o[1], o[2], o[3]}; pq[hh][1] = u4{o[4], o[5], o[6], o[7]}; pq[hh][2] = u4{o[8], 0u, 0u, 0u};
    asm volatile("" : "+v"(pq[hh][0]), "+v"(pq[hh][1]), "+v"(pq[hh][2]));
  };
  phi_q(0); phi_q(1);
  bar_all();
  STAMP(6);
  if (w == 0 && l < 8) st_flag(fl + 8 + 8 * l + m, rq);
  phi_q(2); phi_q(3);
  STAMP(7);
  if (w == 0) {
    for (int spin = 0; spin < (1 << 22); ++spin) {
      if (__builtin_amdgcn_ballot_w64(ld_flag(fl + 8 + 8 * m + (l & 7)) != nonce) == 0) break;
      rq = ld_flag(fl + (l & 7));
      if (l < 8) st_flag(fl + 8 + 8 * l + m, rq);
      __builtin_amdgcn_s_sleep(4);
    }
  }
  bar();
  STAMP(8);
  if (w == 0) {
    rq = ld_flag(fl + (l & 7));
    if (l < 8) st_flag(fl + 8 + 8 * l + m, rq);
  }
  {
    const int hh = w >> 1, hd = w & 1;
    const int reg = (r32 & 3) + 4 * (r32 >> 3), c = reg < 9 ? 9 * ((r32 >> 2) & 1) + reg + 2 * (hh & 1) : 20;
    const unsigned o = KV_OFF + ((hg * 64 + 2 * hh + h) * 3) * 1024 + (c + 32 * hd) * 16;
    u4 t[3][8];
#pragma unroll
    for (int a = 0; a < 3; ++a)
#pragma unroll
      for (int mm = 0; mm < 8; ++mm)
        t[a][mm] = __builtin_amdgcn_raw_buffer_load_b128(rws, (a == 2 && hd) ? 0x80000000u : o, mm * 24576 + a * 1024, 16);
    SB();
#pragma unroll
    for (int a = 0; a < 3; ++a) {
      f4 s = {};
#pragma unroll
      for (int mm = 0; mm < 8; ++mm) s += __builtin_bit_cast(f4, t[a][mm]);
      *(LDS u2*)(sm + L_KVF + (hh * 3 + a) * 1024 + l * 16 + 8 * hd) = u2{pk2(s[0], s[1]), pk2(s[2], s[3])};
    }
  }
  bar_lds();
  STAMP(9);
  float ov[36];
#pragma unroll
  for (int hh = 0; hh < 4; ++hh) {
    f16v o = {};
#pragma unroll
    for (int s = 0; s < 3; ++s) o = mfma32(*(LDS u4*)(sm + L_KVF + (hh * 3 + s) * 1024 + l * 16), pq[hh][s], o);
    const float rd = __builtin_amdgcn_rcpf(o[9] + 1e-6f);
#pragma unroll
    for (int i = 0; i < 9; ++i) ov[9 * hh + i] = o[i] * rd;
  }
  const unsigned ob = OP_OFF + ((16 * m + 2 * w + (r32 >> 4)) * 18 * 64 + (r32 & 15)) * 16;
#pragma unroll
  for (int i = 0; i < 4; ++i)
    __builtin_amdgcn_raw_buffer_store_b128(
        u4{pk2(ov[8 * i], ov[8 * i + 1]), pk2(ov[8 * i + 2], ov[8 * i + 3]), pk2(ov[8 * i + 4], ov[8 * i + 5]), pk2(ov[8 * i + 6], ov[8 * i + 7])},
        rws, ob + (9 * hg + 4 * h + i) * 256, 0, ST_AUX);
  __builtin_amdgcn_raw_buffer_store_b64(u2{pk2(ov[32], ov[33]), pk2(ov[34], ov[35])}, rws, ob + (9 * hg + 8) * 256 + 8 * h, 0, ST_AUX);
  STAMP(10); STAMP_OUT(ws, 0);
  }
}

__global__ void __launch_bounds__(512, 2)
k1(const char* __restrict__ x, const char* __restrict__ wqkv, const unsigned short* __restrict__ wphi,
   const char* __restrict__ wo, const char* __restrict__ wg, const char* __restrict__ wu,
   const char* __restrict__ wd, char* ws) {
  __shared__ __attribute__((aligned(1024))) char smem[163840];
  if ((blockIdx.x >> 5) < 6) k1_body<true>((LDS char*)smem, x, wqkv, wphi, wo, wg, wu, wd, ws);
  else k1_body<false>((LDS char*)smem, x, wqkv, wphi, wo, wg, wu, wd, ws);
}

__global__ void __launch_bounds__(512, 2)
k2(const char* __restrict__ x, char* ws, char* __restrict__ out) {
  __shared__ __attribute__((aligned(1024))) char smem[157696];
  LDS char* sm = (LDS char*)smem;
  const unsigned sb = (unsigned)(uintptr_t)sm;
  const int l = threadIdx.x & 63, w = __builtin_amdgcn_readfirstlane(threadIdx.x >> 6);
  const int xcd = blockIdx.x & 7, bi = blockIdx.x >> 3, q = xcd & 3;
  const int MT = 8 * (xcd >> 2) + (bi & 7), NT = bi >> 3, G = MT * 4 + NT;
  const rsrc_t rpx = __builtin_amdgcn_make_buffer_rsrc(ws + PX_OFF, 0, PX_SZ, 0x00020000);
  unsigned* fl = (unsigned*)(ws + F2_OFF) + G * 64;
  STAMP_DECL STAMP(0);
  constexpr int SLOT = 17408, L_OWN = 8 * SLOT;
  const int lw = w & 3, kq = l >> 4, hf = w >> 2;
  const unsigned vl = l * 16, hl = (kq * 8 + (l & 7)) * 16;
  const unsigned xo = (128 * MT + 64 * (q >> 1) + 16 * lw + (l & 15)) * 1152 + (2 * NT + (q & 1)) * 144;
  const char* fH = ws + HP_OFF + ((8 * MT + lw) * 144 + 36 * q) * 1024;
  const char* fW = ws + WP_OFF + (2 * NT * 162 + 18 + 36 * q) * 4608;
  const char* sW = ws + WP_OFF + (2 * NT + (q & 1)) * (162 * 4608);
  const char* sO = ws + OP_OFF + (8 * MT + 4 * (q >> 1) + lw) * 18432;
  auto issue = [&](auto UU) {
    constexpr int U = decltype(UU)::v;
    const unsigned d = sb + (U % 8) * SLOT + lw * 1024;
    if constexpr (U < 36) {
      dma16(d, vl, fH + U * 1024);
      dma16(d + 8192, vl, fW + U * 4608 + lw * 1024);
      dma16(d + 4096, vl, fH + U * 1024 + 4 * 144 * 1024);
      dma16(d + 12288, vl, fW + (162 + U) * 4608 + lw * 1024);
      dma16_if<4 + (U & 1)>(w, sb + (U % 8) * SLOT + 16384, (l >> 5) * (162 * 4608) + 4096 + (l & 31) * 16, fW + U * 4608);
    } else if constexpr (U < 45) {
#pragma unroll
      for (int u = 0; u < 2; ++u) {
        dma16(d + u * 8192, vl, sO + (2 * (U - 36) + u) * 1024);
        dma16(d + u * 8192 + 4096, vl, sW + (2 * (U - 36) + u) * 4608 + lw * 1024);
      }
      dma16_if<4 + (U & 1)>(w, sb + (U % 8) * SLOT + 16384, (l >> 5) * 4608 + 4096 + (l & 31) * 16, sW + 2 * (U - 36) * 4608);
    }
  };
  if (hf) {
    sfor<0, 7>(issue);
    wait_vm<24>();
    bar();
    sfor<0, 18>([&](auto JJ) {
      constexpr int J = decltype(JJ)::v;
      if (w < 6) wait_vm<18>(); else wait_vm<16>();
      bar();
      issue(IC<2 * J + 7>{}); issue(IC<2 * J + 8>{});
    });
    wait_vm<0>();
    bar();
    issue(IC<43>{});
  } else {
    LDS char* rB = sm + (w >> 1) * 4096 + vl;
    LDS char* rA = sm + 8192 + (w & 1) * 4096 + vl;
    LDS char* rH = sm + 16384 + (w & 1) * 512 + hl;
    f4 acc[4][5];
    u4 A[2][5], B[2][4];
    auto rd = [&](auto UU, auto RR) {
      constexpr int U = decltype(UU)::v, r = decltype(RR)::v, o = (U % 8) * SLOT;
      if constexpr (U < 36) {
        if constexpr (r == 8) A[U % 2][4] = *(LDS u4*)(rH + o);
        else if constexpr (r & 1) A[U % 2][r / 2] = *(LDS u4*)(rA + o + (r / 2) * 1024);
        else B[U % 2][r / 2] = *(LDS u4*)(rB + o + (r / 2) * 1024);
      }
    };
    bar();
    sfor<0, 9>([&](auto RR) { rd(IC<0>{}, RR); });
    sfor<0, 36>([&](auto UU) {
      constexpr int U = decltype(UU)::v;
      if constexpr (U % 12 == 2) STAMP(1 + U / 12);
      if constexpr (U % 2 == 0) bar_lds();
      sfor<0, 20>([&](auto MM) {
        constexpr int n = decltype(MM)::v, i = n / 5, f = n % 5;
        acc[i][f] = mfma16(A[U % 2][f], B[U % 2][i], U == 0 ? f4{} : acc[i][f]);
        if constexpr (n % 2 == 0 && n < 18) rd(IC<U + 1>{}, IC<n / 2>{});
        SB();
      });
    });
    STAMP(4);
    if (w == q) {
#pragma unroll
      for (int i = 0; i < 4; ++i) {
#pragma unroll
        for (int f = 0; f < 4; ++f) *(LDS f4*)(sm + L_OWN + i * 4608 + f * 1024 + vl) = acc[i][f];
        if (l < 32) *(LDS f4*)(sm + L_OWN + i * 4608 + 4096 + vl) = acc[i][4];
      }
    } else {
      const unsigned d = ((G * 4 + w) * 4 + q) * 9216;
#pragma unroll
      for (int i = 0; i < 4; ++i) {
#pragma unroll
        for (int P = 0; P < 2; ++P) {
          const f4 a0 = acc[i][2 * P], a1 = acc[i][2 * P + 1];
          __builtin_amdgcn_raw_buffer_store_b128(u4{pk2(a0[0], a0[1]), pk2(a0[2], a0[3]), pk2(a1[0], a1[1]), pk2(a1[2], a1[3])},
                                                 rpx, d + i * 2304 + P * 1024 + vl, 0, 16);
        }
        __builtin_amdgcn_raw_buffer_store_b64(u2{pk2(acc[i][4][0], acc[i][4][1]), pk2(acc[i][4][2], acc[i][4][3])}, rpx,
                                              l < 32 ? d + i * 2304 + 2048 + l * 8 : 0x80000000u, 0, 16);
      }
    }
    bar_all();
    if (w == 0 && l < 3) st_flag(fl + ((q + 1 + l) & 3) * 16 + q, 1u);
  }
  f4 sa[3] = {};
  auto s_unit = [&](int U) {
    LDS char* b = sm + (U % 8) * SLOT;
#pragma unroll
    for (int u = 0; u < 2; ++u) {
      const u4 Bo = *(LDS u4*)(b + u * 8192 + lw * 1024 + vl);
      sa[0] = mfma16(*(LDS u4*)(b + u * 8192 + 4096 + hf * 2048 + vl), Bo, sa[0]);
      sa[1] = mfma16(*(LDS u4*)(b + u * 8192 + 5120 + hf * 2048 + vl), Bo, sa[1]);
      sa[2] = mfma16(*(LDS u4*)(b + 16384 + u * 512 + hl), Bo, sa[2]);
    }
  };
#pragma unroll
  for (int U = 36; U < 43; ++U) s_unit(U);
  bar_all();
  if (hf) issue(IC<44>{});
  const u4 xv = *(const u4*)(x + xo + 64 * hf + 16 * kq);
  const u2 xh = *(const u2*)(x + xo + 128 + 8 * (kq & 1));
  s_unit(43);
  STAMP(5);
  if (w == 0) {
    for (int spin = 0; spin < (1 << 22); ++spin) {
      const unsigned e = ld_flag(fl + q * 16 + ((q + 1 + (l < 3 ? l : 0)) & 3));
      if (__builtin_amdgcn_ballot_w64(e != 1u) == 0) break;
      __builtin_amdgcn_s_sleep(2);
    }
  }
  bar_all();
  STAMP(6);
  u4 r[3]; u2 rh[3];
#pragma unroll
  for (int j = 0; j < 3; ++j) {
    const unsigned so = ((G * 4 + q) * 4 + ((q + 1 + j) & 3)) * 9216 + lw * 2304;
    r[j] = __builtin_amdgcn_raw_buffer_load_b128(rpx, hf * 1024 + vl, so, 16);
    rh[j] = __builtin_amdgcn_raw_buffer_load_b64(rpx, hf ? 2048 + (l & 31) * 8 : 0x80000000u, so, 16);
  }
  const unsigned po = lw * 4608 + hf * 2048 + vl, ph = lw * 4608 + 4096 + (l & 31) * 16;
  f4 g[3] = {*(LDS f4*)(sm + L_OWN + po), *(LDS f4*)(sm + L_OWN + po + 1024), *(LDS f4*)(sm + L_OWN + ph)};
  s_unit(44);
#pragma unroll
  for (int j = 0; j < 3; ++j) {
    g[0] += f4{blo(r[j][0]), bhi(r[j][0]), blo(r[j][1]), bhi(r[j][1])};
    g[1] += f4{blo(r[j][2]), bhi(r[j][2]), blo(r[j][3]), bhi(r[j][3])};
    g[2] += f4{blo(rh[j][0]), bhi(rh[j][0]), blo(rh[j][1]), bhi(rh[j][1])};
  }
  STAMP(7);
  *(u4*)(out + xo + 64 * hf + 16 * kq) =
      u4{pk2(blo(xv[0]) + silu_mul(g[0][0], sa[0][0]), bhi(xv[0]) + silu_mul(g[0][1], sa[0][1])),
         pk2(blo(xv[1]) + silu_mul(g[0][2], sa[0][2]), bhi(xv[1]) + silu_mul(g[0][3], sa[0][3])),
         pk2(blo(xv[2]) + silu_mul(g[1][0], sa[1][0]), bhi(xv[2]) + silu_mul(g[1][1], sa[1][1])),
         pk2(blo(xv[3]) + silu_mul(g[1][2], sa[1][2]), bhi(xv[3]) + silu_mul(g[1][3], sa[1][3]))};
  if (hf && kq < 2)
    *(u2*)(out + xo + 128 + 8 * kq) = u2{pk2(blo(xh[0]) + silu_mul(g[2][0], sa[2][0]), bhi(xh[0]) + silu_mul(g[2][1], sa[2][1])),
                                         pk2(blo(xh[1]) + silu_mul(g[2][2], sa[2][2]), bhi(xh[1]) + silu_mul(g[2][3], sa[2][3]))};
  STAMP(8); STAMP_OUT(ws, 1);
}

extern "C" void kernel_launch(void* const* d_in, const int* in_sizes, int n_in, void* d_out, int out_size,
                              void* d_ws, size_t ws_size, hipStream_t stream) {
  k1<<<256, 512, 0, stream>>>((const char*)d_in[0], (const char*)d_in[1], (const unsigned short*)d_in[2],
                              (const char*)d_in[3], (const char*)d_in[5], (const char*)d_in[6],
                              (const char*)d_in[7], (char*)d_ws);
  k2<<<256, 512, 0, stream>>>((const char*)d_in[0], (char*)d_ws, (char*)d_out);
}
```

```cpp
#define DMA_ALL
#include <hip/hip_runtime.h>
#include <hip/hip_bf16.h>
#include <stdint.h>
#include <utility>

typedef short s4 __attribute__((ext_vector_type(4)));
typedef __bf16 b2 __attribute__((ext_vector_type(2)));
typedef __bf16 b8 __attribute__((ext_vector_type(8)));
typedef float f4 __attribute__((ext_vector_type(4)));
typedef float f16v __attribute__((ext_vector_type(16)));
typedef unsigned u4 __attribute__((ext_vector_type(4)));
typedef unsigned u2 __attribute__((ext_vector_type(2)));
#define LDS __attribute__((address_space(3)))
#define GLB __attribute__((address_space(1)))
#define DEV __device__ __forceinline__
typedef __amdgpu_buffer_rsrc_t rsrc_t;

#define ST_AUX 16

constexpr unsigned HP_OFF = 0, HP_SZ = 128u * 144u * 1024u;
constexpr unsigned OP_OFF = HP_OFF + HP_SZ, OP_SZ = 128u * 18u * 1024u;
constexpr unsigned WP_OFF = OP_OFF + OP_SZ, WP_SZ = 8u * 162u * 4608u;
constexpr unsigned KV_OFF = WP_OFF + WP_SZ, KV_SZ = 8u * 8u * 8u * 3072u;
constexpr unsigned FL_OFF = KV_OFF + KV_SZ;
constexpr unsigned F2_OFF = FL_OFF + 4096;
constexpr unsigned PX_OFF = F2_OFF + 16384, PX_SZ = 64u * 16u * 9216u;

constexpr int L_X = 32768, L_V = 98304, L_WPHI = 141312, L_KVF = 149504, VS = 168, PS = 328;

DEV unsigned pk2(float a, float b) { b2 q = {(__bf16)a, (__bf16)b}; return __builtin_bit_cast(unsigned, q); }
DEV float asf(unsigned v) { return __builtin_bit_cast(float, v); }
DEV b2 asb2(unsigned v) { return __builtin_bit_cast(b2, v); }
DEV float blo(unsigned v) { return __builtin_bit_cast(float, v << 16); }
DEV float bhi(unsigned v) { return __builtin_bit_cast(float, v & 0xffff0000u); }
DEV f16v mfma32(u4 a, u4 b, f16v c) {
  return __builtin_amdgcn_mfma_f32_32x32x16_bf16(__builtin_bit_cast(b8, a), __builtin_bit_cast(b8, b), c, 0, 0, 0);
}
DEV f4 mfma16(u4 a, u4 b, f4 c) {
  return __builtin_amdgcn_mfma_f32_16x16x32_bf16(__builtin_bit_cast(b8, a), __builtin_bit_cast(b8, b), c, 0, 0, 0);
}
DEV u2 trr(LDS char* p) { return __builtin_bit_cast(u2, __builtin_amdgcn_ds_read_tr16_b64_v4i16((LDS s4*)p)); }
DEV u4 trr2(LDS char* p, int d) { u2 a = trr(p), b = trr(p + d); return u4{a.x, a.y, b.x, b.y}; }
DEV float xadd(float v) {
  auto r = __builtin_amdgcn_permlane32_swap(__builtin_bit_cast(unsigned, v), __builtin_bit_cast(unsigned, v), false, false);
  return asf(r[0]) + asf(r[1]);
}
DEV float xadd16(float v) {
  auto r = __builtin_amdgcn_permlane16_swap(__builtin_bit_cast(unsigned, v), __builtin_bit_cast(unsigned, v), false, false);
  return asf(r[0]) + asf(r[1]);
}
DEV float xmax(float v) {
  auto r = __builtin_amdgcn_permlane32_swap(__builtin_bit_cast(unsigned, v), __builtin_bit_cast(unsigned, v), false, false);
  return fmaxf(asf(r[0]), asf(r[1]));
}
DEV void dma16(unsigned ldsdst, unsigned voff, const void* sbase) {
  unsigned keep;
  asm volatile("s_mov_b32 %0, m0\n\ts_mov_b32 m0, %1\n\ts_nop 0\n\tglobal_load_lds_dwordx4 %2, %3\n\ts_mov_b32 m0, %0"
               : "=&s"(keep) : "s"(ldsdst), "v"(voff), "s"(sbase) : "memory");
}
template <int K> DEV void dma16_if(int w, unsigned ldsdst, unsigned voff, const void* sbase) {
  unsigned keep;
  asm volatile("s_cmp_lg_u32 %4, %5\n\ts_cbranch_scc1 .Lskip%=\n\ts_mov_b32 %0, m0\n\ts_mov_b32 m0, %1\n\ts_nop 0\n\t"
               "global_load_lds_dwordx4 %2, %3\n\ts_mov_b32 m0, %0\n.Lskip%=:"
               : "=&s"(keep) : "s"(ldsdst), "v"(voff), "s"(sbase), "s"(w), "i"(K) : "memory", "scc");
}
template <int N> DEV void wait_vm() { asm volatile("s_waitcnt vmcnt(%0)" ::"i"(N) : "memory"); }
template <int N> DEV void wait_lgkm() { asm volatile("s_waitcnt lgkmcnt(%0)" ::"i"(N) : "memory"); }
DEV int lane_fresh() {
  int v;
  asm volatile("v_mbcnt_lo_u32_b32 %0, -1, 0\n\tv_mbcnt_hi_u32_b32 %0, -1, %0" : "=v"(v));
  return v;
}
DEV void bar() { asm volatile("s_barrier" ::: "memory"); }
DEV void bar_all() { asm volatile("s_waitcnt vmcnt(0) lgkmcnt(0)\n\ts_barrier" ::: "memory"); }
DEV void bar_lds() { asm volatile("s_waitcnt lgkmcnt(0)\n\ts_barrier" ::: "memory"); }
#define SB() __builtin_amdgcn_sched_barrier(0)
#define STAMP_DECL
#define STAMP(i)
#define STAMP_OUT(ws, k)
template <int I> struct IC { static constexpr int v = I; };
template <int B, class Fn, int... I> DEV void sfor_(Fn&& f, std::integer_sequence<int, I...>) { (f(IC<B + I>{}), ...); }
template <int B, int E, class Fn> DEV void sfor(Fn&& f) { sfor_<B>(f, std::make_integer_sequence<int, E - B>{}); }
DEV float silu_mul(float g, float u) {
  float t = __builtin_amdgcn_exp2f(g * -1.4426950408889634f);
  return (g * __builtin_amdgcn_rcpf(1.0f + t)) * u;
}

#define PFD 4
#define SYNC0 2
#define NPRE 1
#define LOOK 6
constexpr int n_slabs(int mode) { return mode == 0 ? 27 : 18; }
constexpr int n_ring(int mode) { return mode == 0 ? 9 : 4; }
constexpr bool is_sync(int mode, int g) { return mode == 1 || g <= SYNC0 || g % 3 == 0 || g == 25; }
constexpr int next_sync(int mode, int g) { int x = g + 1; while (!is_sync(mode, x)) ++x; return x; }
constexpr int iss_at(int mode, int s) {
  const int la = mode == 0 ? LOOK : 3;
  return s < 2 ? -2 : (s < la ? -1 : s - la);
}
constexpr int w_ops(int mode, int g) { int n = 0; for (int s = 2; s < n_slabs(mode); ++s) if (iss_at(mode, s) == g) n += 2; return n; }
constexpr int ex_in(int mode, int g) {
  return mode == 0 ? ((g % 9 == 8 || (g % 9 == 0 && g > 0)) ? 2 : ((g == 21 || g == 25) ? 5 : 0)) : 0;
}
constexpr int vm_before(int mode, int g) {
  int s = next_sync(mode, g), n = 63;
  if (s >= n_slabs(mode)) s = n_slabs(mode) - 1;
  if (s >= 2 && iss_at(mode, s) < g) {
    n = 0;
    for (int t = s + 1; t < n_slabs(mode); ++t) if (iss_at(mode, t) < g) n += 2;
    for (int x = iss_at(mode, s) + 1; x < g; ++x) n += ex_in(mode, x);
  }
  if (mode == 0 && g == 25) n = 0;
  return n;
}

struct Acc16 { f4 v[2][2]; };
template <int MODE, class Acc, class Iss, class Hook>
DEV void stream(LDS char* sm, u4 (&xf)[36], Acc (&acc)[4], unsigned (&ra)[4], Iss&& issue, Hook&& hook) {
  constexpr int NS = n_slabs(MODE);
  constexpr int NSTEP = MODE == 0 ? 27 * 16 : 9 * 16 + 9 * 12;
  u4 wf[PFD + 1];
  auto rd = [&](auto NN) {
    constexpr int n = decltype(NN)::v;
    if constexpr (n < NSTEP) {
      constexpr bool p2 = (MODE == 1 && n >= 144);
      constexpr int g = p2 ? 9 + (n - 144) / 12 : n / 16;
      constexpr int i = p2 ? (n - 144) % 12 : n % 16;
      if constexpr (MODE == 0 && g > 0 && g % 3 == 0 && i % 4 == 0) {
        ra[i / 4] += (g % 9 == 0) ? -98304 : 49152;
        asm volatile("" : "+v"(ra[i / 4]));
      }
      wf[n % (PFD + 1)] = trr2(sm + ra[i / 4] + (MODE == 0 ? (g % 3) * 16384 + (i & 1) * 8192 + (i & 2) * 2048 : (g % 4) * 16384 + (i % 4) * 2048), 512);
    }
  };
  sfor<0, PFD>(rd);
  sfor<0, NS>([&](auto GG) { sfor<0, ((MODE == 1 && decltype(GG)::v >= 9) ? 12 : 16)>([&](auto II) {
    constexpr int g = decltype(GG)::v, i = decltype(II)::v;
    constexpr int n = (MODE == 1 && g >= 9) ? 144 + (g - 9) * 12 + i : g * 16 + i;
    constexpr int t = i / 4, sl = g % 9;
    if constexpr (i == 0 && g > 0 && is_sync(MODE, g)) {
      if constexpr (vm_before(MODE, g) < 63) wait_vm<vm_before(MODE, g)>();
      wait_lgkm<2 * PFD>();
      bar();
    }
    sfor<2, NS>([&](auto SS) { if constexpr (iss_at(MODE, decltype(SS)::v) == g) issue(SS, II, IC<((MODE == 1 && g >= 9) ? 4 : 8)>{}); });
    rd(IC<n + PFD>{});
    hook(GG, II);
    if constexpr (MODE == 0) {
#pragma unroll
      for (int tb = 0; tb < 2; ++tb)
        acc[t].v[i & 1][tb] = mfma16(wf[n % (PFD + 1)], xf[sl * 4 + (i & 2) + tb], (sl == 0 && i % 4 < 2) ? f4{} : acc[t].v[i & 1][tb]);
    } else if constexpr (sl == 0 && i % 4 == 0) acc[t] = mfma32(wf[n % (PFD + 1)], xf[sl * 4 + i % 4], f16v{});
    else acc[t] = mfma32(wf[n % (PFD + 1)], xf[sl * 4 + i % 4], acc[t]);
    SB();
  }); });
  if constexpr (MODE == 1) asm volatile("" : "+v"(acc[0]), "+v"(acc[1]), "+v"(acc[2]));
}

DEV void softmax2(const f16v& p, unsigned (&o)[9]) {
  float mx = p[0], mn = p[0];
#pragma unroll
  for (int i = 1; i < 9; ++i) { mx = fmaxf(mx, p[i]); mn = fminf(mn, p[i]); }
  mx = xmax(mx); mn = -xmax(-mn);
  const float L = 1.4426950408889634f;
  float e[18], s1 = 0.f, s2 = 0.f, a = mx * L, b = mn * L;
#pragma unroll
  for (int i = 0; i < 9; ++i) {
    e[i] = __builtin_amdgcn_exp2f(fmaf(p[i], L, -a)); s1 += e[i];
    e[9 + i] = __builtin_amdgcn_exp2f(fmaf(p[i], -L, b)); s2 += e[9 + i];
  }
  s1 = __builtin_amdgcn_rcpf(xadd(s1)); s2 = __builtin_amdgcn_rcpf(xadd(s2));
#pragma unroll
  for (int i = 0; i < 9; ++i) { e[i] *= s1; e[9 + i] *= s2; }
#pragma unroll
  for (int i = 0; i < 9; ++i) o[i] = pk2(e[2 * i], e[2 * i + 1]);
}
DEV u4 pack8(const f16v& a, int o) {
  return u4{pk2(a[o], a[o + 1]), pk2(a[o + 2], a[o + 3]), pk2(a[o + 4], a[o + 5]), pk2(a[o + 6], a[o + 7])};
}
DEV unsigned ld_flag(const unsigned* p) { return __hip_atomic_load((GLB unsigned*)p, __ATOMIC_RELAXED, __HIP_MEMORY_SCOPE_AGENT); }
DEV void st_flag(unsigned* p, unsigned v) { __hip_atomic_store((GLB unsigned*)p, v, __ATOMIC_RELAXED, __HIP_MEMORY_SCOPE_AGENT); }

template <bool ffn>
DEV void k1_body(LDS char* sm, const char* __restrict__ x, const char* __restrict__ wqkv, const unsigned short* __restrict__ wphi,
   const char* __restrict__ wo, const char* __restrict__ wg, const char* __restrict__ wu,
   const char* __restrict__ wd, char* ws) {
  STAMP_DECL STAMP(0);
  const unsigned sb = (unsigned)(uintptr_t)sm;
  const int l = threadIdx.x & 63, w = __builtin_amdgcn_readfirstlane(threadIdx.x >> 6);
  const int xcd = blockIdx.x & 7, bi = blockIdx.x >> 3;
  const int m = 2 * (xcd & 3) + (bi & 1), cc = bi >> 1;
  const int cg = 12 * (xcd >> 2) + cc;
  const int hg = 4 * (xcd >> 2) + (cc - 12);
  const int r32 = l & 31, h = l >> 5;
  const rsrc_t rws = __builtin_amdgcn_make_buffer_rsrc(ws, 0, FL_OFF, 0x00020000);

  const int drow = l >> 3, dpc = l & 7, ds0 = (drow >> 1) & 1;
  const int uG = (dpc >> 1) ^ ds0 ^ (ffn ? 2 * (w & 1) : 0), uU = ffn ? uG : uG ^ 2;
  unsigned vG, vU;
  const char *bG, *bU;
  auto qcol = [&](int pass, int f0) {
    return pass == 0 ? (f0 < 72 ? 576 + f0 : 1152 + f0 - 72) : (f0 < 16 ? 1152 + 56 + f0 : (f0 < 88 ? f0 - 16 : 0));
  };
  unsigned nonce = 0, wp[8];
  unsigned* fl = (unsigned*)(ws + FL_OFF) + (ffn ? 0 : hg) * 128;
  if constexpr (ffn) {
    vG = (8 * w + drow) * 9216 + uG * 32 + (dpc & 1) * 16;
    vU = (8 * w + drow) * 9216 + uU * 32 + (dpc & 1) * 16;
    bG = wg + cg * 384; bU = wu + cg * 384;
  } else {
    vG = (8 * w + drow) * 3456 + 2 * qcol(0, 32 * uG + 8 * (dpc & 1));
    vU = (8 * w + drow) * 3456 + 2 * qcol(0, 32 * uU + 16 + 8 * (dpc & 1));
    bG = bU = wqkv + hg * 144;
    const int reg = (r32 & 3) + 4 * (r32 >> 3), f = 9 * ((r32 >> 2) & 1) + reg, hh = w >> 1;
#pragma unroll
    for (int j = 0; j < 8; ++j) {
      int d = 16 * (hh + (w & 1)) + 8 * (j >> 2) + 4 * h + (j & 3) - 18 * hh;
      bool ok = reg < 9 && d >= 0 && d < 18;
      unsigned v = wphi[(4 * hg + hh) * 324 + (ok ? d * 18 + f : 0)];
      wp[j] = ok ? v : 0u;
    }
    if (w == 0) {
      unsigned v = ld_flag(fl + (l < 8 ? 8 + 8 * m + l : m));
      while (__builtin_amdgcn_ballot_w64(v == nonce) != 0) ++nonce;
      if (l == 0) st_flag(fl + m, nonce);
    }
  }
  if (threadIdx.x >= 496) st_flag((unsigned*)(ws + F2_OFF) + blockIdx.x * 16 + (l & 15), 0u);
  const unsigned wdst = sb + w * 1024;
  u4 xf[36];
  float s1[2] = {}, s2[2] = {};
  {
    const unsigned xv0 = (256 * m + 32 * w + drow) * 1152 + 16 * (dpc ^ (l >> 4));
    const unsigned xv1 = (256 * m + 32 * w + 8 + drow) * 1152 + 16 * (dpc ^ (4 + (l >> 4)));
    const unsigned xd = sb + L_X + w * 16384;
    auto xiss = [&](int c) {
      dma16(xd + (c & 3) * 4096, xv0, x + c * 128);
      dma16(xd + (c & 3) * 4096 + 1024, xv1, x + c * 128);
      dma16(xd + (c & 3) * 4096 + 2048, xv0, x + c * 128 + 16 * 1152);
      dma16(xd + (c & 3) * 4096 + 3072, xv1, x + c * 128 + 16 * 1152);
    };
    xiss(0); xiss(1); xiss(2); xiss(3);
    const unsigned rs = ffn ? 64 * 9216 : 64 * 3456;
    dma16(wdst, vG, bG); dma16(wdst + 8192, vU, bU);
    dma16(wdst + 16384, vG, bG + rs); dma16(wdst + 16384 + 8192, vU, bU + rs);
    LDS char* xr = sm + L_X + w * 16384 + (ffn ? l & 15 : r32) * 128;
    const b2 one = {(__bf16)1.0f, (__bf16)1.0f};
    sfor<0, 9>([&](auto C) {
      constexpr int c = decltype(C)::v;
      wait_vm<(c < 4 ? 16 : (c < 6 ? 12 : 4 * (8 - c)))>();
#pragma unroll
      for (int s = 0; s < 4; ++s)
        xf[4 * c + s] = *(LDS u4*)(xr + (c & 3) * 4096 + (ffn ? (s & 1) * 2048 + 16 * ((2 * (s & 2) + (l >> 4)) ^ ((l >> 1) & 7))
                                                              : 16 * ((2 * s + h) ^ ((r32 >> 1) & 7))));
      if constexpr (c + 4 < 9) { wait_lgkm<0>(); xiss(c + 4); }
#pragma unroll
      for (int s = 0; s < 4; ++s)
#pragma unroll
        for (int e = 0; e < 4; ++e) {
          b2 v = asb2(xf[4 * c + s][e]);
          s1[ffn ? s & 1 : 0] = __builtin_amdgcn_fdot2_f32_bf16(v, one, s1[ffn ? s & 1 : 0], false);
          s2[ffn ? s & 1 : 0] = __builtin_amdgcn_fdot2_f32_bf16(v, v, s2[ffn ? s & 1 : 0], false);
        }
      SB();
    });
  }
  STAMP(1);
  bar_all();
  STAMP(2);
  float rstd[2], nmr[2];
#pragma unroll
  for (int t = 0; t < (ffn ? 2 : 1); ++t) {
    const float mu = xadd(ffn ? xadd16(s1[t]) : s1[t]) * (1.0f / 576.0f), e2 = xadd(ffn ? xadd16(s2[t]) : s2[t]) * (1.0f / 576.0f);
    rstd[t] = __builtin_amdgcn_rsqf(fmaxf(e2 - mu * mu, 0.f) + 1e-5f); nmr[t] = -mu * rstd[t];
  }
  auto norm4 = [&](int i) {
    const float a = rstd[ffn ? i & 1 : 0], b = nmr[ffn ? i & 1 : 0];
    auto n1 = [&](unsigned v) { return pk2(fmaf(blo(v), a, b), fmaf(bhi(v), a, b)); };
    asm volatile("" : "+v"(xf[i]));
    xf[i] = u4{n1(xf[i][0]), n1(xf[i][1]), n1(xf[i][2]), n1(xf[i][3])};
  };
  unsigned ra[4];
  {
    const int g = (l >> 4) & 1, q = (l >> 2) & 3, p = l & 3;
#pragma unroll
    for (int t = 0; t < 4; ++t) ra[t] = (ffn ? (l >> 4) * 1024 : g * 8192 + h * 1024) + q * 128 + 32 * (t ^ (2 * g + (q >> 1))) + 8 * p;
  }

  if constexpr (ffn) {
    Acc16 acc[4];
    auto issue = [&](auto G, auto I, auto) {
      constexpr int g = decltype(G)::v, i = decltype(I)::v;
      constexpr unsigned o = (g % 9) * 64u * 9216u + (g / 9) * 128u;
      if constexpr (i == 2) dma16(wdst + (g % 9) * 16384, vG, bG + o);
      if constexpr (i == 10) dma16(wdst + (g % 9) * 16384 + 8192, vU, bU + o);
    };
#pragma unroll
    for (int s = 0; s < 4 * NPRE; ++s) {
      if (s < LOOK - 2) {
        dma16(wdst + (s + 2) * 16384, vG, bG + (s + 2) * 64u * 9216u);
        dma16(wdst + (s + 2) * 16384 + 8192, vU, bU + (s + 2) * 64u * 9216u);
      }
      norm4(s);
    }
    const unsigned hv = HP_OFF + (16 * m + 2 * w) * 144 * 1024 + l * 16 + cg * 6144;
    unsigned hq[2][4];
    auto epi = [&](int tile, int pass, int e) {
      const int tb = e >> 1, o = 2 * (e & 1);
      const f4 &g = acc[tile].v[0][tb], &u = acc[tile].v[1][tb];
      hq[tb][2 * (tile & 1) + (e & 1)] = pk2(silu_mul(g[o], u[o]), silu_mul(g[o + 1], u[o + 1]));
      if ((tile & 1) && (e & 1))
        __builtin_amdgcn_raw_buffer_store_b128(u4{hq[tb][0], hq[tb][1], hq[tb][2], hq[tb][3]}, rws,
                                               hv + tb * 144 * 1024 + (2 * pass + (tile >> 1)) * 1024, 0, ST_AUX);
    };
    const int ks = min((int)blockIdx.x, 161);
    auto rt_load = [&](int e) {
      const int it = min(w + 8 * e, 35);
      const int P = 64 * it + lane_fresh(), rho = P / 72, pc = P - 72 * rho, kq = rho >> 3, j = rho & 7;
      int row = 32 * (ks - 18) + 16 * (j >> 2) + 4 * kq + (j & 3);
      if (ks < 18) {
        const int u = 4 * ks + kq, g9 = u / 9, uu = u - 9 * g9;
        const int idx = uu < 8 ? 8 * (uu & 3) + j : 32 + (j & 3), hf = uu < 8 ? uu >> 2 : j >> 2;
        const int hd = idx / 9;
        row = 18 * (4 * g9 + hd) + 9 * hf + idx - 9 * hd;
      }
      dma16(sb + it * 1024, row * 1152 + pc * 16, ks < 18 ? wo : wd);
    };
    auto rt_store = [&](int f) {
      const int l = lane_fresh(), kq = l >> 4, p = l & 3;
      LDS char* b = sm + (8 * kq + ((l >> 2) & 3)) * 1152 + 144 * w;
      const unsigned dst = WP_OFF + (w * 162 + ks) * 4608;
      if (f < 4)
        __builtin_amdgcn_raw_buffer_store_b128(trr2(b + 64 * (f >> 1) + 16 * p + 8 * (f & 1), 4608), rws, dst + f * 1024 + l * 16, 0, ST_AUX);
      else
        __builtin_amdgcn_raw_buffer_store_b128(trr2(b + 128 + 8 * (p & 1), 4608), rws,
                                               (l & 8) ? 0x80000000u : dst + 4096 + (kq * 8 + (l & 7)) * 16, 0, ST_AUX);
    };
    stream<0>(sm, xf, acc, ra, issue, [&](auto G, auto I) {
      constexpr int g = decltype(G)::v, i = decltype(I)::v;
      if constexpr (g == 21 && i % 2 == 0 && i < 10) rt_load(i / 2);
      if constexpr (g == 25 && i % 2 == 0 && i < 10) rt_store(i / 2);
      if constexpr (g + NPRE < 9 && i % 4 == 1) norm4(4 * (g + NPRE) + i / 4);
      if constexpr (g % 9 == 0 && i == 0) STAMP(3 + g / 9);
      if constexpr (g % 9 == 8 && i >= 6) epi((i - 6) / 4, g / 9, (i - 6) % 4);
      if constexpr (g % 9 == 0 && g > 0 && i < 6) epi((i + 10) / 4, g / 9 - 1, (i + 10) % 4);
    });
    epi(2, 2, 2); epi(2, 2, 3);
#pragma unroll
    for (int e = 0; e < 4; ++e) epi(3, 2, e);
    STAMP(6); STAMP_OUT(ws, 0);
  } else {
  f16v acc[4];
  {
    auto issue = [&](auto G, auto I, auto D) {
      constexpr int g = decltype(G)::v, i = decltype(I)::v, d = decltype(D)::v;
      if constexpr (g == 9 && i == 0) {
        const int l = lane_fresh(), drow = l >> 3, dpc = l & 7, uG = (dpc >> 1) ^ ((drow >> 1) & 1), uU = uG ^ 2;
        vG = (8 * w + drow) * 3456 + 2 * qcol(1, 32 * uG + 8 * (dpc & 1));
        vU = (8 * w + drow) * 3456 + 2 * qcol(1, 32 * uU + 16 + 8 * (dpc & 1));
      }
      constexpr unsigned o = (g % 9) * 64u * 3456u;
      if constexpr (i == 0 || i == 2) dma16_if<i / 2>(w >> 2, wdst + (g % 4) * 16384, vG, bG + o);
      if constexpr (i == d || i == d + 2) dma16_if<(i - d) / 2>(w >> 2, wdst + (g % 4) * 16384 + 8192, vU, bG + o);
    };
    *(LDS u4*)(sm + L_WPHI + w * 1024 + l * 16) =
        u4{wp[0] | wp[1] << 16, wp[2] | wp[3] << 16, wp[4] | wp[5] << 16, wp[6] | wp[7] << 16};
    dma16(wdst + 32768, vG, bG + 128u * 3456u);
    dma16(wdst + 32768 + 8192, vU, bG + 128u * 3456u);
#pragma unroll
    for (int s = 0; s < 4 * NPRE; ++s) norm4(s);
    stream<1>(sm, xf, acc, ra, issue, [&](auto G, auto I) {
      constexpr int g = decltype(G)::v, i = decltype(I)::v;
      if constexpr (g + NPRE < 9 && i % 4 == 1) norm4(4 * (g + NPRE) + i / 4);
      if constexpr (g % 9 == 0 && i == 0) STAMP(3 + g / 9);
      if constexpr (g == 9 && i == 0) {
        const int l = lane_fresh(), h = l >> 5;
        LDS char* vrow = sm + L_V + (32 * w + (l & 31)) * VS;
        if (h == 0) *(LDS u2*)(vrow + 144) = u2{0x3f80u, 0u};
#pragma unroll
        for (int a = 0; a < 4; ++a) *(LDS u4*)(sm + 65536 + (w * 4 + a) * 1024 + l * 16) = pack8(acc[a / 2], 8 * (a & 1));
        *(LDS u2*)(sm + L_KVF + w * 512 + l * 8) = u2{pk2(acc[2][0], acc[2][1]), pk2(acc[2][2], acc[2][3])};
#pragma unroll
        for (int a = 1; a < 8; ++a) {
          const f16v& c = acc[2 + a / 4];
          const int o = 4 * (a & 3);
          *(LDS u2*)(vrow + 16 * (a - 1) + 8 * h) = u2{pk2(c[o], c[o + 1]), pk2(c[o + 2], c[o + 3])};
        }
        wait_lgkm<0>();
      }
    });
  }
  const int l = lane_fresh(), r32 = l & 31, h = l >> 5;
  LDS char* vrow = sm + L_V + (32 * w + r32) * VS;
#pragma unroll
  for (int a = 0; a < 2; ++a)
    *(LDS u2*)(vrow + 112 + 16 * a + 8 * h) = u2{pk2(acc[0][4 * a], acc[0][4 * a + 1]), pk2(acc[0][4 * a + 2], acc[0][4 * a + 3])};
  u4 qf[5] = {pack8(acc[0], 8), pack8(acc[1], 0), pack8(acc[1], 8), pack8(acc[2], 0), pack8(acc[2], 8)};
  u4 kf[5];
#pragma unroll
  for (int a = 0; a < 4; ++a) kf[a] = *(LDS u4*)(sm + 65536 + (w * 4 + a) * 1024 + l * 16);
  { u2 t = *(LDS u2*)(sm + L_KVF + w * 512 + l * 8); kf[4] = u4{t.x, t.y, 0u, 0u}; }
  bar_lds();
  STAMP(5);
  u4 wpf[8];
#pragma unroll
  for (int i = 0; i < 8; ++i) wpf[i] = *(LDS u4*)(sm + L_WPHI + i * 1024 + l * 16);
#pragma unroll
  for (int hh = 0; hh < 4; ++hh) {
    f16v p = mfma32(wpf[2 * hh], kf[hh], f16v{});
    p = mfma32(wpf[2 * hh + 1], kf[hh + 1], p);
    unsigned o[9];
    softmax2(p, o);
    LDS char* d = sm + (32 * w + r32) * PS + (2 * hh + h) * 40;
#pragma unroll
    for (int i = 0; i < 4; ++i) *(LDS u2*)(d + 8 * i) = u2{o[2 * i], o[2 * i + 1]};
    *(LDS u2*)(d + 32) = u2{o[8], 0u};
  }
  bar_lds();
  {
    const int hh = w >> 1, pi = 4 * ((l >> 4) & 1) + (l & 3), tq = 8 * h + ((l >> 2) & 3);
    LDS char* pa = sm + tq * PS + w * 40 + 8 * (pi < 4 ? pi : 4);
    LDS char* pb = sm + L_V + tq * VS + (pi < 5 ? 2 * (18 * hh - 2 * (hh & 1)) + 8 * pi : 144);
    f16v kv = {};
#pragma unroll
    for (int s = 0; s < 16; ++s) kv = mfma32(trr2(pa + s * 16 * PS, 4 * PS), trr2(pb + s * 16 * VS, 4 * VS), kv);
    if (r32 <= 20) {
#pragma unroll
      for (int a = 0; a < 3; ++a)
        __builtin_amdgcn_raw_buffer_store_b128(
            __builtin_bit_cast(u4, f4{kv[4 * a], kv[4 * a + 1], kv[4 * a + 2], kv[4 * a + 3]}), rws,
            KV_OFF + (((hg * 8 + m) * 8 + w) * 3 + a) * 1024 + l * 16, 0, 16);
    }
  }
  unsigned rq = 0;
  if (w == 0) rq = ld_flag(fl + (l & 7));
  u4 pq[4][3];
  auto phi_q = [&](int hh) {
    f16v p = mfma32(wpf[2 * hh], qf[hh], f16v{});
    p = mfma32(wpf[2 * hh + 1], qf[hh + 1], p);
    unsigned o[9];
    softmax2(p, o);
    pq[hh][0] = u4{o[0], o[1], o[2], o[3]}; pq[hh][1] = u4{o[4], o[5], o[6], o[7]}; pq[hh][2] = u4{o[8], 0u, 0u, 0u};
    asm volatile("" : "+v"(pq[hh][0]), "+v"(pq[hh][1]), "+v"(pq[hh][2]));
  };
  phi_q(0); phi_q(1);
  bar_all();
  STAMP(6);
  if (w == 0 && l < 8) st_flag(fl + 8 + 8 * l + m, rq);
  phi_q(2); phi_q(3);
  STAMP(7);
  if (w == 0) {
    for (int spin = 0; spin < (1 << 22); ++spin) {
      if (__builtin_amdgcn_ballot_w64(ld_flag(fl + 8 + 8 * m + (l & 7)) != nonce) == 0) break;
      rq = ld_flag(fl + (l & 7));
      if (l < 8) st_flag(fl + 8 + 8 * l + m, rq);
      __builtin_amdgcn_s_sleep(4);
    }
  }
  bar();
  STAMP(8);
  if (w == 0) {
    rq = ld_flag(fl + (l & 7));
    if (l < 8) st_flag(fl + 8 + 8 * l + m, rq);
  }
  {
    const int hh = w >> 1, hd = w & 1;
    const int reg = (r32 & 3) + 4 * (r32 >> 3), c = reg < 9 ? 9 * ((r32 >> 2) & 1) + reg + 2 * (hh & 1) : 20;
    const unsigned o = KV_OFF + ((hg * 64 + 2 * hh + h) * 3) * 1024 + (c + 32 * hd) * 16;
    u4 t[3][8];
#pragma unroll
    for (int a = 0; a < 3; ++a)
#pragma unroll
      for (int mm = 0; mm < 8; ++mm)
        t[a][mm] = __builtin_amdgcn_raw_buffer_load_b128(rws, (a == 2 && hd) ? 0x80000000u : o, mm * 24576 + a * 1024, 16);
    SB();
#pragma unroll
    for (int a = 0; a < 3; ++a) {
      f4 s = {};
#pragma unroll
      for (int mm = 0; mm < 8; ++mm) s += __builtin_bit_cast(f4, t[a][mm]);
      *(LDS u2*)(sm + L_KVF + (hh * 3 + a) * 1024 + l * 16 + 8 * hd) = u2{pk2(s[0], s[1]), pk2(s[2], s[3])};
    }
  }
  bar_lds();
  STAMP(9);
  float ov[36];
#pragma unroll
  for (int hh = 0; hh < 4; ++hh) {
    f16v o = {};
#pragma unroll
    for (int s = 0; s < 3; ++s) o = mfma32(*(LDS u4*)(sm + L_KVF + (hh * 3 + s) * 1024 + l * 16), pq[hh][s], o);
    const float rd = __builtin_amdgcn_rcpf(o[9] + 1e-6f);
#pragma unroll
    for (int i = 0; i < 9; ++i) ov[9 * hh + i] = o[i] * rd;
  }
  const unsigned ob = OP_OFF + ((16 * m + 2 * w + (r32 >> 4)) * 18 * 64 + (r32 & 15)) * 16;
#pragma unroll
  for (int i = 0; i < 4; ++i)
    __builtin_amdgcn_raw_buffer_store_b128(
        u4{pk2(ov[8 * i], ov[8 * i + 1]), pk2(ov[8 * i + 2], ov[8 * i + 3]), pk2(ov[8 * i + 4], ov[8 * i + 5]), pk2(ov[8 * i + 6], ov[8 * i + 7])},
        rws, ob + (9 * hg + 4 * h + i) * 256, 0, ST_AUX);
  __builtin_amdgcn_raw_buffer_store_b64(u2{pk2(ov[32], ov[33]), pk2(ov[34], ov[35])}, rws, ob + (9 * hg + 8) * 256 + 8 * h, 0, ST_AUX);
  STAMP(10); STAMP_OUT(ws, 0);
  }
}

__global__ void __launch_bounds__(512, 2)
k1(const char* __restrict__ x, const char* __restrict__ wqkv, const unsigned short* __restrict__ wphi,
   const char* __restrict__ wo, const char* __restrict__ wg, const char* __restrict__ wu,
   const char* __restrict__ wd, char* ws) {
  __shared__ __attribute__((aligned(1024))) char smem[163840];
  if ((blockIdx.x >> 5) < 6) k1_body<true>((LDS char*)smem, x, wqkv, wphi, wo, wg, wu, wd, ws);
  else k1_body<false>((LDS char*)smem, x, wqkv, wphi, wo, wg, wu, wd, ws);
}

__global__ void __launch_bounds__(512, 2)
k2(const char* __restrict__ x, char* ws, char* __restrict__ out) {
  __shared__ __attribute__((aligned(1024))) char smem[157696];
  LDS char* sm = (LDS char*)smem;
  const unsigned sb = (unsigned)(uintptr_t)sm;
  const int l = threadIdx.x & 63, w = __builtin_amdgcn_readfirstlane(threadIdx.x >> 6);
  const int xcd = blockIdx.x & 7, bi = blockIdx.x >> 3, q = xcd & 3;
  const int MT = 8 * (xcd >> 2) + (bi & 7), NT = bi >> 3, G = MT * 4 + NT;
  const rsrc_t rpx = __builtin_amdgcn_make_buffer_rsrc(ws + PX_OFF, 0, PX_SZ, 0x00020000);
  unsigned* fl = (unsigned*)(ws + F2_OFF) + G * 64;
  STAMP_DECL STAMP(0);
  constexpr int SLOT = 17408, L_OWN = 8 * SLOT;
  const int lw = w & 3, kq = l >> 4, hf = w >> 2;
  const unsigned vl = l * 16, hl = (kq * 8 + (l & 7)) * 16;
  const unsigned xo = (128 * MT + 64 * (q >> 1) + 16 * lw + (l & 15)) * 1152 + (2 * NT + (q & 1)) * 144;
  const char* fH = ws + HP_OFF + ((8 * MT + lw) * 144 + 36 * q) * 1024;
  const char* fW = ws + WP_OFF + (2 * NT * 162 + 18 + 36 * q) * 4608;
  const char* sW = ws + WP_OFF + (2 * NT + (q & 1)) * (162 * 4608);
  const char* sO = ws + OP_OFF + (8 * MT + 4 * (q >> 1) + lw) * 18432;
  auto issue = [&](auto UU) {
    constexpr int U = decltype(UU)::v;
    const unsigned d = sb + (U % 8) * SLOT + lw * 1024;
    if constexpr (U < 36) {
      dma16(d, vl, fH + U * 1024);
      dma16(d + 8192, vl, fW + U * 4608 + lw * 1024);
      dma16(d + 4096, vl, fH + U * 1024 + 4 * 144 * 1024);
      dma16(d + 12288, vl, fW + (162 + U) * 4608 + lw * 1024);
      dma16_if<4 + (U & 1)>(w, sb + (U % 8) * SLOT + 16384, (l >> 5) * (162 * 4608) + 4096 + (l & 31) * 16, fW + U * 4608);
    } else if constexpr (U < 45) {
#pragma unroll
      for (int u = 0; u < 2; ++u) {
        dma16(d + u * 8192, vl, sO + (2 * (U - 36) + u) * 1024);
        dma16(d + u * 8192 + 4096, vl, sW + (2 * (U - 36) + u) * 4608 + lw * 1024);
      }
      dma16_if<4 + (U & 1)>(w, sb + (U % 8) * SLOT + 16384, (l >> 5) * 4608 + 4096 + (l & 31) * 16, sW + 2 * (U - 36) * 4608);
    }
  };
  if (hf) {
    sfor<0, 7>(issue);
    wait_vm<24>();
    bar();
    sfor<0, 18>([&](auto JJ) {
      constexpr int J = decltype(JJ)::v;
      if (w < 6) wait_vm<18>(); else wait_vm<16>();
      bar();
      issue(IC<2 * J + 7>{}); issue(IC<2 * J + 8>{});
    });
    wait_vm<0>();
    bar();
    issue(IC<43>{});
  } else {
    LDS char* rB = sm + (w >> 1) * 4096 + vl;
    LDS char* rA = sm + 8192 + (w & 1) * 4096 + vl;
    LDS char* rH = sm + 16384 + (w & 1) * 512 + hl;
    f4 acc[4][5];
    u4 A[2][5], B[2][4];
    auto rd = [&](auto UU, auto RR) {
      constexpr int U = decltype(UU)::v, r = decltype(RR)::v, o = (U % 8) * SLOT;
      if constexpr (U < 36) {
        if constexpr (r == 8) A[U % 2][4] = *(LDS u4*)(rH + o);
        else if constexpr (r & 1) A[U % 2][r / 2] = *(LDS u4*)(rA + o + (r / 2) * 1024);
        else B[U % 2][r / 2] = *(LDS u4*)(rB + o + (r / 2) * 1024);
      }
    };
    bar();
    sfor<0, 9>([&](auto RR) { rd(IC<0>{}, RR); });
    sfor<0, 36>([&](auto UU) {
      constexpr int U = decltype(UU)::v;
      if constexpr (U % 12 == 2) STAMP(1 + U / 12);
      if constexpr (U % 2 == 0) bar_lds();
      sfor<0, 20>([&](auto MM) {
        constexpr int n = decltype(MM)::v, i = n / 5, f = n % 5;
        acc[i][f] = mfma16(A[U % 2][f], B[U % 2][i], U == 0 ? f4{} : acc[i][f]);
        if constexpr (n % 2 == 0 && n < 18) rd(IC<U + 1>{}, IC<n / 2>{});
        SB();
      });
    });
    STAMP(4);
    if (w == q) {
#pragma unroll
      for (int i = 0; i < 4; ++i) {
#pragma unroll
        for (int f = 0; f < 4; ++f) *(LDS f4*)(sm + L_OWN + i * 4608 + f * 1024 + vl) = acc[i][f];
        if (l < 32) *(LDS f4*)(sm + L_OWN + i * 4608 + 4096 + vl) = acc[i][4];
      }
    } else {
      const unsigned d = ((G * 4 + w) * 4 + q) * 9216;
#pragma unroll
      for (int i = 0; i < 4; ++i) {
#pragma unroll
        for (int P = 0; P < 2; ++P) {
          const f4 a0 = acc[i][2 * P], a1 = acc[i][2 * P + 1];
          __builtin_amdgcn_raw_buffer_store_b128(u4{pk2(a0[0], a0[1]), pk2(a0[2], a0[3]), pk2(a1[0], a1[1]), pk2(a1[2], a1[3])},
                                                 rpx, d + i * 2304 + P * 1024 + vl, 0, 16);
        }
        __builtin_amdgcn_raw_buffer_store_b64(u2{pk2(acc[i][4][0], acc[i][4][1]), pk2(acc[i][4][2], acc[i][4][3])}, rpx,
                                              l < 32 ? d + i * 2304 + 2048 + l * 8 : 0x80000000u, 0, 16);
      }
    }
    bar_all();
    if (w == 0 && l < 3) st_flag(fl + ((q + 1 + l) & 3) * 16 + q, 1u);
  }
  f4 sa[3] = {};
  auto s_unit = [&](int U) {
    LDS char* b = sm + (U % 8) * SLOT;
#pragma unroll
    for (int u = 0; u < 2; ++u) {
      const u4 Bo = *(LDS u4*)(b + u * 8192 + lw * 1024 + vl);
      sa[0] = mfma16(*(LDS u4*)(b + u * 8192 + 4096 + hf * 2048 + vl), Bo, sa[0]);
      sa[1] = mfma16(*(LDS u4*)(b + u * 8192 + 5120 + hf * 2048 + vl), Bo, sa[1]);
      sa[2] = mfma16(*(LDS u4*)(b + 16384 + u * 512 + hl), Bo, sa[2]);
    }
  };
#pragma unroll
  for (int U = 36; U < 43; ++U) s_unit(U);
  bar_all();
  if (hf) issue(IC<44>{});
  const u4 xv = *(const u4*)(x + xo + 64 * hf + 16 * kq);
  const u2 xh = *(const u2*)(x + xo + 128 + 8 * (kq & 1));
  s_unit(43);
  STAMP(5);
  if (w == 0) {
    for (int spin = 0; spin < (1 << 22); ++spin) {
      const unsigned e = ld_flag(fl + q * 16 + ((q + 1 + (l < 3 ? l : 0)) & 3));
      if (__builtin_amdgcn_ballot_w64(e != 1u) == 0) break;
      __builtin_amdgcn_s_sleep(2);
    }
  }
  bar_all();
  STAMP(6);
  u4 r[3]; u2 rh[3];
#pragma unroll
  for (int j = 0; j < 3; ++j) {
    const unsigned so = ((G * 4 + q) * 4 + ((q + 1 + j) & 3)) * 9216 + lw * 2304;
    r[j] = __builtin_amdgcn_raw_buffer_load_b128(rpx, hf * 1024 + vl, so, 16);
    rh[j] = __builtin_amdgcn_raw_buffer_load_b64(rpx, hf ? 2048 + (l & 31) * 8 : 0x80000000u, so, 16);
  }
  const unsigned po = lw * 4608 + hf * 2048 + vl, ph = lw * 4608 + 4096 + (l & 31) * 16;
  f4 g[3] = {*(LDS f4*)(sm + L_OWN + po), *(LDS f4*)(sm + L_OWN + po + 1024), *(LDS f4*)(sm + L_OWN + ph)};
  s_unit(44);
#pragma unroll
  for (int j = 0; j < 3; ++j) {
    g[0] += f4{blo(r[j][0]), bhi(r[j][0]), blo(r[j][1]), bhi(r[j][1])};
    g[1] += f4{blo(r[j][2]), bhi(r[j][2]), blo(r[j][3]), bhi(r[j][3])};
    g[2] += f4{blo(rh[j][0]), bhi(rh[j][0]), blo(rh[j][1]), bhi(rh[j][1])};
  }
  STAMP(7);
  *(u4*)(out + xo + 64 * hf + 16 * kq) =
      u4{pk2(blo(xv[0]) + silu_mul(g[0][0], sa[0][0]), bhi(xv[0]) + silu_mul(g[0][1], sa[0][1])),
         pk2(blo(xv[1]) + silu_mul(g[0][2], sa[0][2]), bhi(xv[1]) + silu_mul(g[0][3], sa[0][3])),
         pk2(blo(xv[2]) + silu_mul(g[1][0], sa[1][0]), bhi(xv[2]) + silu_mul(g[1][1], sa[1][1])),
         pk2(blo(xv[3]) + silu_mul(g[1][2], sa[1][2]), bhi(xv[3]) + silu_mul(g[1][3], sa[1][3]))};
  if (hf && kq < 2)
    *(u2*)(out + xo + 128 + 8 * kq) = u2{pk2(blo(xh[0]) + silu_mul(g[2][0], sa[2][0]), bhi(xh[0]) + silu_mul(g[2][1], sa[2][1])),
                                         pk2(blo(xh[1]) + silu_mul(g[2][2], sa[2][2]), bhi(xh[1]) + silu_mul(g[2][3], sa[2][3]))};
  STAMP(8); STAMP_OUT(ws, 1);
}

extern "C" void kernel_launch(void* const* d_in, const int* in_sizes, int n_in, void* d_out, int out_size,
                              void* d_ws, size_t ws_size, hipStream_t stream) {
  k1<<<256, 512, 0, stream>>>((const char*)d_in[0], (const char*)d_in[1], (const unsigned short*)d_in[2],
                              (const char*)d_in[3], (const char*)d_in[5], (const char*)d_in[6],
                              (const char*)d_in[7], (char*)d_ws);
  k2<<<256, 512, 0, stream>>>((const char*)d_in[0], (char*)d_ws, (char*)d_out);
}
```

```cpp
#include <hip/hip_runtime.h>
#include <hip/hip_bf16.h>
#include <stdint.h>
#include <utility>

typedef short s4 __attribute__((ext_vector_type(4)));
typedef __bf16 b2 __attribute__((ext_vector_type(2)));
typedef __bf16 b8 __attribute__((ext_vector_type(8)));
typedef float f4 __attribute__((ext_vector_type(4)));
typedef float f16v __attribute__((ext_vector_type(16)));
typedef unsigned u4 __attribute__((ext_vector_type(4)));
typedef unsigned u2 __attribute__((ext_vector_type(2)));
#define LDS __attribute__((address_space(3)))
#define GLB __attribute__((address_space(1)))
#define DEV __device__ __forceinline__
typedef __amdgpu_buffer_rsrc_t rsrc_t;

#define ST_AUX 16

constexpr unsigned HP_OFF = 0, HP_SZ = 128u * 144u * 1024u;
constexpr unsigned OP_OFF = HP_OFF + HP_SZ, OP_SZ = 128u * 18u * 1024u;
constexpr unsigned WP_OFF = OP_OFF + OP_SZ, WP_SZ = 8u * 162u * 4608u;
constexpr unsigned KV_OFF = WP_OFF + WP_SZ, KV_SZ = 8u * 8u * 8u * 3072u;
constexpr unsigned FL_OFF = KV_OFF + KV_SZ;
constexpr unsigned F2_OFF = FL_OFF + 4096;
constexpr unsigned PX_OFF = F2_OFF + 16384, PX_SZ = 64u * 16u * 9216u;

constexpr int L_X = 32768, L_V = 98304, L_WPHI = 141312, L_KVF = 149504, VS = 168, PS = 328;

DEV unsigned pk2(float a, float b) { b2 q = {(__bf16)a, (__bf16)b}; return __builtin_bit_cast(unsigned, q); }
DEV float asf(unsigned v) { return __builtin_bit_cast(float, v); }
DEV b2 asb2(unsigned v) { return __builtin_bit_cast(b2, v); }
DEV float blo(unsigned v) { return __builtin_bit_cast(float, v << 16); }
DEV float bhi(unsigned v) { return __builtin_bit_cast(float, v & 0xffff0000u); }
DEV f16v mfma32(u4 a, u4 b, f16v c) {
  return __builtin_amdgcn_mfma_f32_32x32x16_bf16(__builtin_bit_cast(b8, a), __builtin_bit_cast(b8, b), c, 0, 0, 0);
}
DEV f4 mfma16(u4 a, u4 b, f4 c) {
  return __builtin_amdgcn_mfma_f32_16x16x32_bf16(__builtin_bit_cast(b8, a), __builtin_bit_cast(b8, b), c, 0, 0, 0);
}
DEV u2 trr(LDS char* p) { return __builtin_bit_cast(u2, __builtin_amdgcn_ds_read_tr16_b64_v4i16((LDS s4*)p)); }
DEV u4 trr2(LDS char* p, int d) { u2 a = trr(p), b = trr(p + d); return u4{a.x, a.y, b.x, b.y}; }
DEV float xadd(float v) {
  auto r = __builtin_amdgcn_permlane32_swap(__builtin_bit_cast(unsigned, v), __builtin_bit_cast(unsigned, v), false, false);
  return asf(r[0]) + asf(r[1]);
}
DEV float xadd16(float v) {
  auto r = __builtin_amdgcn_permlane16_swap(__builtin_bit_cast(unsigned, v), __builtin_bit_cast(unsigned, v), false, false);
  return asf(r[0]) + asf(r[1]);
}
DEV float xmax(float v) {
  auto r = __builtin_amdgcn_permlane32_swap(__builtin_bit_cast(unsigned, v), __builtin_bit_cast(unsigned, v), false, false);
  return fmaxf(asf(r[0]), asf(r[1]));
}
DEV void dma16(unsigned ldsdst, unsigned voff, const void* sbase) {
  unsigned keep;
  asm volatile("s_mov_b32 %0, m0\n\ts_mov_b32 m0, %1\n\ts_nop 0\n\tglobal_load_lds_dwordx4 %2, %3\n\ts_mov_b32 m0, %0"
               : "=&s"(keep) : "s"(ldsdst), "v"(voff), "s"(sbase) : "memory");
}
template <int K> DEV void dma16_if(int w, unsigned ldsdst, unsigned voff, const void* sbase) {
  unsigned keep;
  asm volatile("s_cmp_lg_u32 %4, %5\n\ts_cbranch_scc1 .Lskip%=\n\ts_mov_b32 %0, m0\n\ts_mov_b32 m0, %1\n\ts_nop 0\n\t"
               "global_load_lds_dwordx4 %2, %3\n\ts_mov_b32 m0, %0\n.Lskip%=:"
               : "=&s"(keep) : "s"(ldsdst), "v"(voff), "s"(sbase), "s"(w), "i"(K) : "memory", "scc");
}
template <int N> DEV void wait_vm() { asm volatile("s_waitcnt vmcnt(%0)" ::"i"(N) : "memory"); }
template <int N> DEV void wait_lgkm() { asm volatile("s_waitcnt lgkmcnt(%0)" ::"i"(N) : "memory"); }
DEV int lane_fresh() {
  int v;
  asm volatile("v_mbcnt_lo_u32_b32 %0, -1, 0\n\tv_mbcnt_hi_u32_b32 %0, -1, %0" : "=v"(v));
  return v;
}
DEV void bar() { asm volatile("s_barrier" ::: "memory"); }
DEV void bar_all() { asm volatile("s_waitcnt vmcnt(0) lgkmcnt(0)\n\ts_barrier" ::: "memory"); }
DEV void bar_lds() { asm volatile("s_waitcnt lgkmcnt(0)\n\ts_barrier" ::: "memory"); }
#define SB() __builtin_amdgcn_sched_barrier(0)
template <int I> struct IC { static constexpr int v = I; };
template <int B, class Fn, int... I> DEV void sfor_(Fn&& f, std::integer_sequence<int, I...>) { (f(IC<B + I>{}), ...); }
template <int B, int E, class Fn> DEV void sfor(Fn&& f) { sfor_<B>(f, std::make_integer_sequence<int, E - B>{}); }
DEV float silu_mul(float g, float u) {
  float t = __builtin_amdgcn_exp2f(g * -1.4426950408889634f);
  return (g * __builtin_amdgcn_rcpf(1.0f + t)) * u;
}

#define PFD 4
#define SYNC0 2
#define NPRE 1
#define LOOK 6
constexpr int n_slabs(int mode) { return mode == 0 ? 27 : 18; }
constexpr int n_ring(int mode) { return mode == 0 ? 9 : 4; }
constexpr bool is_sync(int mode, int g) { return mode == 1 || g <= SYNC0 || g % 3 == 0 || g == 25; }
constexpr int next_sync(int mode, int g) { int x = g + 1; while (!is_sync(mode, x)) ++x; return x; }
constexpr int iss_at(int mode, int s) {
  const int la = mode == 0 ? LOOK : 3;
  return s < 2 ? -2 : (s < la ? -1 : s - la);
}
constexpr int w_ops(int mode, int g) { int n = 0; for (int s = 2; s < n_slabs(mode); ++s) if (iss_at(mode, s) == g) n += 2; return n; }
constexpr int ex_in(int mode, int g) {
  return mode == 0 ? ((g % 9 == 8 || (g % 9 == 0 && g > 0)) ? 2 : ((g == 21 || g == 25) ? 5 : 0)) : 0;
}
constexpr int vm_before(int mode, int g) {
  int s = next_sync(mode, g), n = 63;
  if (s >= n_slabs(mode)) s = n_slabs(mode) - 1;
  if (s >= 2 && iss_at(mode, s) < g) {
    n = 0;
    for (int t = s + 1; t < n_slabs(mode); ++t) if (iss_at(mode, t) < g) n += 2;
    for (int x = iss_at(mode, s) + 1; x < g; ++x) n += ex_in(mode, x);
  }
  if (mode == 0 && g == 25) n = 0;
  return n;
}

struct Acc16 { f4 v[2][2]; };
template <int MODE, class Acc, class Iss, class Hook>
DEV void stream(LDS char* sm, u4 (&xf)[36], Acc (&acc)[4], unsigned (&ra)[4], Iss&& issue, Hook&& hook) {
  constexpr int NS = n_slabs(MODE);
  constexpr int NSTEP = MODE == 0 ? 27 * 16 : 9 * 16 + 9 * 12;
  u4 wf[PFD + 1];
  auto rd = [&](auto NN) {
    constexpr int n = decltype(NN)::v;
    if constexpr (n < NSTEP) {
      constexpr bool p2 = (MODE == 1 && n >= 144);
      constexpr int g = p2 ? 9 + (n - 144) / 12 : n / 16;
      constexpr int i = p2 ? (n - 144) % 12 : n % 16;
      if constexpr (MODE == 0 && g > 0 && g % 3 == 0 && i % 4 == 0) {
        ra[i / 4] += (g % 9 == 0) ? -98304 : 49152;
        asm volatile("" : "+v"(ra[i / 4]));
      }
      wf[n % (PFD + 1)] = trr2(sm + ra[i / 4] + (MODE == 0 ? (g % 3) * 16384 + (i & 1) * 8192 + (i & 2) * 2048 : (g % 4) * 16384 + (i % 4) * 2048), 512);
    }
  };
  sfor<0, PFD>(rd);
  sfor<0, NS>([&](auto GG) { sfor<0, ((MODE == 1 && decltype(GG)::v >= 9) ? 12 : 16)>([&](auto II) {
    constexpr int g = decltype(GG)::v, i = decltype(II)::v;
    constexpr int n = (MODE == 1 && g >= 9) ? 144 + (g - 9) * 12 + i : g * 16 + i;
    constexpr int t = i / 4, sl = g % 9;
    if constexpr (i == 0 && g > 0 && is_sync(MODE, g)) {
      if constexpr (vm_before(MODE, g) < 63) wait_vm<vm_before(MODE, g)>();
      wait_lgkm<2 * PFD>();
      bar();
    }
    sfor<2, NS>([&](auto SS) { if constexpr (iss_at(MODE, decltype(SS)::v) == g) issue(SS, II, IC<((MODE == 1 && g >= 9) ? 4 : 8)>{}); });
    rd(IC<n + PFD>{});
    hook(GG, II);
    if constexpr (MODE == 0) {
#pragma unroll
      for (int tb = 0; tb < 2; ++tb)
        acc[t].v[i & 1][tb] = mfma16(wf[n % (PFD + 1)], xf[sl * 4 + (i & 2) + tb], (sl == 0 && i % 4 < 2) ? f4{} : acc[t].v[i & 1][tb]);
    } else if constexpr (sl == 0 && i % 4 == 0) acc[t] = mfma32(wf[n % (PFD + 1)], xf[sl * 4 + i % 4], f16v{});
    else acc[t] = mfma32(wf[n % (PFD + 1)], xf[sl * 4 + i % 4], acc[t]);
    SB();
  }); });
  if constexpr (MODE == 1) asm volatile("" : "+v"(acc[0]), "+v"(acc[1]), "+v"(acc[2]));
}

DEV void softmax2(const f16v& p, unsigned (&o)[9]) {
  float mx = p[0], mn = p[0];
#pragma unroll
  for (int i = 1; i < 9; ++i) { mx = fmaxf(mx, p[i]); mn = fminf(mn, p[i]); }
  mx = xmax(mx); mn = -xmax(-mn);
  const float L = 1.4426950408889634f;
  float e[18], s1 = 0.f, s2 = 0.f, a = mx * L, b = mn * L;
#pragma unroll
  for (int i = 0; i < 9; ++i) {
    e[i] = __builtin_amdgcn_exp2f(fmaf(p[i], L, -a)); s1 += e[i];
    e[9 + i] = __builtin_amdgcn_exp2f(fmaf(p[i], -L, b)); s2 += e[9 + i];
  }
  s1 = __builtin_amdgcn_rcpf(xadd(s1)); s2 = __builtin_amdgcn_rcpf(xadd(s2));
#pragma unroll
  for (int i = 0; i < 9; ++i) { e[i] *= s1; e[9 + i] *= s2; }
#pragma unroll
  for (int i = 0; i < 9; ++i) o[i] = pk2(e[2 * i], e[2 * i + 1]);
}
DEV u4 pack8(const f16v& a, int o) {
  return u4{pk2(a[o], a[o + 1]), pk2(a[o + 2], a[o + 3]), pk2(a[o + 4], a[o + 5]), pk2(a[o + 6], a[o + 7])};
}
DEV unsigned ld_flag(const unsigned* p) { return __hip_atomic_load((GLB unsigned*)p, __ATOMIC_RELAXED, __HIP_MEMORY_SCOPE_AGENT); }
DEV void st_flag(unsigned* p, unsigned v) { __hip_atomic_store((GLB unsigned*)p, v, __ATOMIC_RELAXED, __HIP_MEMORY_SCOPE_AGENT); }

template <bool ffn>
DEV void k1_body(LDS char* sm, const char* __restrict__ x, const char* __restrict__ wqkv, const unsigned short* __restrict__ wphi,
   const char* __restrict__ wo, const char* __restrict__ wg, const char* __restrict__ wu,
   const char* __restrict__ wd, char* ws) {
  const unsigned sb = (unsigned)(uintptr_t)sm;
  const int l = threadIdx.x & 63, w = __builtin_amdgcn_readfirstlane(threadIdx.x >> 6);
  const int xcd = blockIdx.x & 7, bi = blockIdx.x >> 3;
  const int m = 4 * (xcd & 1) + (bi & 3), cc = bi >> 2;
  const int cg = 6 * (xcd >> 1) + cc;
  const int hg = 2 * (xcd >> 1) + (cc - 6);
  const int r32 = l & 31, h = l >> 5;
  const rsrc_t rws = __builtin_amdgcn_make_buffer_rsrc(ws, 0, FL_OFF, 0x00020000);

  const int drow = l >> 3, dpc = l & 7, ds0 = (drow >> 1) & 1;
  const int uG = (dpc >> 1) ^ ds0 ^ (ffn ? 2 * (w & 1) : 0), uU = ffn ? uG : uG ^ 2;
  unsigned vG, vU;
  const char *bG, *bU;
  auto qcol = [&](int pass, int f0) {
    return pass == 0 ? (f0 < 72 ? 576 + f0 : 1152 + f0 - 72) : (f0 < 16 ? 1152 + 56 + f0 : (f0 < 88 ? f0 - 16 : 0));
  };
  unsigned nonce = 0, wp[8];
  unsigned* fl = (unsigned*)(ws + FL_OFF) + (ffn ? 0 : hg) * 128;
  if constexpr (ffn) {
    vG = (8 * w + drow) * 9216 + uG * 32 + (dpc & 1) * 16;
    vU = (8 * w + drow) * 9216 + uU * 32 + (dpc & 1) * 16;
    bG = wg + cg * 384; bU = wu + cg * 384;
  } else {
    vG = (8 * w + drow) * 3456 + 2 * qcol(0, 32 * uG + 8 * (dpc & 1));
    vU = (8 * w + drow) * 3456 + 2 * qcol(0, 32 * uU + 16 + 8 * (dpc & 1));
    bG = bU = wqkv + hg * 144;
    const int reg = (r32 & 3) + 4 * (r32 >> 3), f = 9 * ((r32 >> 2) & 1) + reg, hh = w >> 1;
#pragma unroll
    for (int j = 0; j < 8; ++j) {
      int d = 16 * (hh + (w & 1)) + 8 * (j >> 2) + 4 * h + (j & 3) - 18 * hh;
      bool ok = reg < 9 && d >= 0 && d < 18;
      unsigned v = wphi[(4 * hg + hh) * 324 + (ok ? d * 18 + f : 0)];
      wp[j] = ok ? v : 0u;
    }
    if (w == 0) {
      unsigned v = ld_flag(fl + (l < 8 ? 8 + 8 * m + l : m));
      while (__builtin_amdgcn_ballot_w64(v == nonce) != 0) ++nonce;
      if (l == 0) st_flag(fl + m, nonce);
    }
  }
  if (threadIdx.x >= 496) st_flag((unsigned*)(ws + F2_OFF) + blockIdx.x * 16 + (l & 15), 0u);
  const unsigned wdst = sb + w * 1024;
  u4 xf[36];
  float s1[2] = {}, s2[2] = {};
  {
    const unsigned xv0 = (256 * m + 32 * w + drow) * 1152 + 16 * (dpc ^ (l >> 4));
    const unsigned xv1 = (256 * m + 32 * w + 8 + drow) * 1152 + 16 * (dpc ^ (4 + (l >> 4)));
    const unsigned xd = sb + L_X + w * 16384;
    auto xiss = [&](int c) {
      dma16(xd + (c & 3) * 4096, xv0, x + c * 128);
      dma16(xd + (c & 3) * 4096 + 1024, xv1, x + c * 128);
      dma16(xd + (c & 3) * 4096 + 2048, xv0, x + c * 128 + 16 * 1152);
      dma16(xd + (c & 3) * 4096 + 3072, xv1, x + c * 128 + 16 * 1152);
    };
    xiss(0); xiss(1); xiss(2); xiss(3);
    const unsigned rs = ffn ? 64 * 9216 : 64 * 3456;
    dma16(wdst, vG, bG); dma16(wdst + 8192, vU, bU);
    dma16(wdst + 16384, vG, bG + rs); dma16(wdst + 16384 + 8192, vU, bU + rs);
    LDS char* xr = sm + L_X + w * 16384 + (ffn ? l & 15 : r32) * 128;
    const b2 one = {(__bf16)1.0f, (__bf16)1.0f};
    sfor<0, 9>([&](auto C) {
      constexpr int c = decltype(C)::v;
      wait_vm<(c < 4 ? 16 : (c < 6 ? 12 : 4 * (8 - c)))>();
#pragma unroll
      for (int s = 0; s < 4; ++s)
        xf[4 * c + s] = *(LDS u4*)(xr + (c & 3) * 4096 + (ffn ? (s & 1) * 2048 + 16 * ((2 * (s & 2) + (l >> 4)) ^ ((l >> 1) & 7))
                                                              : 16 * ((2 * s + h) ^ ((r32 >> 1) & 7))));
      if constexpr (c + 4 < 9) { wait_lgkm<0>(); xiss(c + 4); }
#pragma unroll
      for (int s = 0; s < 4; ++s)
#pragma unroll
        for (int e = 0; e < 4; ++e) {
          b2 v = asb2(xf[4 * c + s][e]);
          s1[ffn ? s & 1 : 0] = __builtin_amdgcn_fdot2_f32_bf16(v, one, s1[ffn ? s & 1 : 0], false);
          s2[ffn ? s & 1 : 0] = __builtin_amdgcn_fdot2_f32_bf16(v, v, s2[ffn ? s & 1 : 0], false);
        }
      SB();
    });
  }
  bar_all();
  float rstd[2], nmr[2];
#pragma unroll
  for (int t = 0; t < (ffn ? 2 : 1); ++t) {
    const float mu = xadd(ffn ? xadd16(s1[t]) : s1[t]) * (1.0f / 576.0f), e2 = xadd(ffn ? xadd16(s2[t]) : s2[t]) * (1.0f / 576.0f);
    rstd[t] = __builtin_amdgcn_rsqf(fmaxf(e2 - mu * mu, 0.f) + 1e-5f); nmr[t] = -mu * rstd[t];
  }
  auto norm4 = [&](int i) {
    const float a = rstd[ffn ? i & 1 : 0], b = nmr[ffn ? i & 1 : 0];
    auto n1 = [&](unsigned v) { return pk2(fmaf(blo(v), a, b), fmaf(bhi(v), a, b)); };
    asm volatile("" : "+v"(xf[i]));
    xf[i] = u4{n1(xf[i][0]), n1(xf[i][1]), n1(xf[i][2]), n1(xf[i][3])};
  };
  unsigned ra[4];
  {
    const int g = (l >> 4) & 1, q = (l >> 2) & 3, p = l & 3;
#pragma unroll
    for (int t = 0; t < 4; ++t) ra[t] = (ffn ? (l >> 4) * 1024 : g * 8192 + h * 1024) + q * 128 + 32 * (t ^ (2 * g + (q >> 1))) + 8 * p;
  }

  if constexpr (ffn) {
    Acc16 acc[4];
    auto issue = [&](auto G, auto I, auto) {
      constexpr int g = decltype(G)::v, i = decltype(I)::v;
      constexpr unsigned o = (g % 9) * 64u * 9216u + (g / 9) * 128u;
      if constexpr (i == 2) dma16(wdst + (g % 9) * 16384, vG, bG + o);
      if constexpr (i == 10) dma16(wdst + (g % 9) * 16384 + 8192, vU, bU + o);
    };
#pragma unroll
    for (int s = 0; s < 4 * NPRE; ++s) {
      if (s < LOOK - 2) {
        dma16(wdst + (s + 2) * 16384, vG, bG + (s + 2) * 64u * 9216u);
        dma16(wdst + (s + 2) * 16384 + 8192, vU, bU + (s + 2) * 64u * 9216u);
      }
      norm4(s);
    }
    const unsigned hv = HP_OFF + (16 * m + 2 * w) * 144 * 1024 + l * 16 + cg * 6144;
    unsigned hq[2][4];
    auto epi = [&](int tile, int pass, int e) {
      const int tb = e >> 1, o = 2 * (e & 1);
      const f4 &g = acc[tile].v[0][tb], &u = acc[tile].v[1][tb];
      hq[tb][2 * (tile & 1) + (e & 1)] = pk2(silu_mul(g[o], u[o]), silu_mul(g[o + 1], u[o + 1]));
      if ((tile & 1) && (e & 1))
        __builtin_amdgcn_raw_buffer_store_b128(u4{hq[tb][0], hq[tb][1], hq[tb][2], hq[tb][3]}, rws,
                                               hv + tb * 144 * 1024 + (2 * pass + (tile >> 1)) * 1024, 0, ST_AUX);
    };
    const int ks = min((int)blockIdx.x, 161);
    auto rt_load = [&](int e) {
      const int it = min(w + 8 * e, 35);
      const int P = 64 * it + lane_fresh(), rho = P / 72, pc = P - 72 * rho, kq = rho >> 3, j = rho & 7;
      int row = 32 * (ks - 18) + 16 * (j >> 2) + 4 * kq + (j & 3);
      if (ks < 18) {
        const int u = 4 * ks + kq, g9 = u / 9, uu = u - 9 * g9;
        const int idx = uu < 8 ? 8 * (uu & 3) + j : 32 + (j & 3), hf = uu < 8 ? uu >> 2 : j >> 2;
        const int hd = idx / 9;
        row = 18 * (4 * g9 + hd) + 9 * hf + idx - 9 * hd;
      }
      dma16(sb + it * 1024, row * 1152 + pc * 16, ks < 18 ? wo : wd);
    };
    auto rt_store = [&](int f) {
      const int l = lane_fresh(), kq = l >> 4, p = l & 3;
      LDS char* b = sm + (8 * kq + ((l >> 2) & 3)) * 1152 + 144 * w;
      const unsigned dst = blockIdx.x > 161 ? 0x80000000u : WP_OFF + (w * 162 + ks) * 4608;
      if (f < 4)
        __builtin_amdgcn_raw_buffer_store_b128(trr2(b + 64 * (f >> 1) + 16 * p + 8 * (f & 1), 4608), rws, dst + f * 1024 + l * 16, 0, ST_AUX);
      else
        __builtin_amdgcn_raw_buffer_store_b128(trr2(b + 128 + 8 * (p & 1), 4608), rws,
                                               (l & 8) ? 0x80000000u : dst + 4096 + (kq * 8 + (l & 7)) * 16, 0, ST_AUX);
    };
    stream<0>(sm, xf, acc, ra, issue, [&](auto G, auto I) {
      constexpr int g = decltype(G)::v, i = decltype(I)::v;
      if constexpr (g == 21 && i % 2 == 0 && i < 10) rt_load(i / 2);
      if constexpr (g == 25 && i % 2 == 0 && i < 10) rt_store(i / 2);
      if constexpr (g + NPRE < 9 && i % 4 == 1) norm4(4 * (g + NPRE) + i / 4);
      if constexpr (g % 9 == 8 && i >= 6) epi((i - 6) / 4, g / 9, (i - 6) % 4);
      if constexpr (g % 9 == 0 && g > 0 && i < 6) epi((i + 10) / 4, g / 9 - 1, (i + 10) % 4);
    });
    epi(2, 2, 2); epi(2, 2, 3);
#pragma unroll
    for (int e = 0; e < 4; ++e) epi(3, 2, e);
  } else {
  f16v acc[4];
  {
    auto issue = [&](auto G, auto I, auto D) {
      constexpr int g = decltype(G)::v, i = decltype(I)::v, d = decltype(D)::v;
      if constexpr (g == 9 && i == 0) {
        const int l = lane_fresh(), drow = l >> 3, dpc = l & 7, uG = (dpc >> 1) ^ ((drow >> 1) & 1), uU = uG ^ 2;
        vG = (8 * w + drow) * 3456 + 2 * qcol(1, 32 * uG + 8 * (dpc & 1));
        vU = (8 * w + drow) * 3456 + 2 * qcol(1, 32 * uU + 16 + 8 * (dpc & 1));
      }
      constexpr unsigned o = (g % 9) * 64u * 3456u;
      if constexpr (i == 0 || i == 2) dma16_if<i / 2>(w >> 2, wdst + (g % 4) * 16384, vG, bG + o);
      if constexpr (i == d || i == d + 2) dma16_if<(i - d) / 2>(w >> 2, wdst + (g % 4) * 16384 + 8192, vU, bG + o);
    };
    *(LDS u4*)(sm + L_WPHI + w * 1024 + l * 16) =
        u4{wp[0] | wp[1] << 16, wp[2] | wp[3] << 16, wp[4] | wp[5] << 16, wp[6] | wp[7] << 16};
    dma16(wdst + 32768, vG, bG + 128u * 3456u);
    dma16(wdst + 32768 + 8192, vU, bG + 128u * 3456u);
#pragma unroll
    for (int s = 0; s < 4 * NPRE; ++s) norm4(s);
    stream<1>(sm, xf, acc, ra, issue, [&](auto G, auto I) {
      constexpr int g = decltype(G)::v, i = decltype(I)::v;
      if constexpr (g + NPRE < 9 && i % 4 == 1) norm4(4 * (g + NPRE) + i / 4);
      if constexpr (g == 9 && i == 0) {
        const int l = lane_fresh(), h = l >> 5;
        LDS char* vrow = sm + L_V + (32 * w + (l & 31)) * VS;
        if (h == 0) *(LDS u2*)(vrow + 144) = u2{0x3f80u, 0u};
#pragma unroll
        for (int a = 0; a < 4; ++a) *(LDS u4*)(sm + 65536 + (w * 4 + a) * 1024 + l * 16) = pack8(acc[a / 2], 8 * (a & 1));
        *(LDS u2*)(sm + L_KVF + w * 512 + l * 8) = u2{pk2(acc[2][0], acc[2][1]), pk2(acc[2][2], acc[2][3])};
#pragma unroll
        for (int a = 1; a < 8; ++a) {
          const f16v& c = acc[2 + a / 4];
          const int o = 4 * (a & 3);
          *(LDS u2*)(vrow + 16 * (a - 1) + 8 * h) = u2{pk2(c[o], c[o + 1]), pk2(c[o + 2], c[o + 3])};
        }
        wait_lgkm<0>();
      }
    });
  }
  const int l = lane_fresh(), r32 = l & 31, h = l >> 5;
  LDS char* vrow = sm + L_V + (32 * w + r32) * VS;
#pragma unroll
  for (int a = 0; a < 2; ++a)
    *(LDS u2*)(vrow + 112 + 16 * a + 8 * h) = u2{pk2(acc[0][4 * a], acc[0][4 * a + 1]), pk2(acc[0][4 * a + 2], acc[0][4 * a + 3])};
  u4 qf[5] = {pack8(acc[0], 8), pack8(acc[1], 0), pack8(acc[1], 8), pack8(acc[2], 0), pack8(acc[2], 8)};
  u4 kf[5];
#pragma unroll
  for (int a = 0; a < 4; ++a) kf[a] = *(LDS u4*)(sm + 65536 + (w * 4 + a) * 1024 + l * 16);
  { u2 t = *(LDS u2*)(sm + L_KVF + w * 512 + l * 8); kf[4] = u4{t.x, t.y, 0u, 0u}; }
  bar_lds();
  u4 wpf[8];
#pragma unroll
  for (int i = 0; i < 8; ++i) wpf[i] = *(LDS u4*)(sm + L_WPHI + i * 1024 + l * 16);
#pragma unroll
  for (int hh = 0; hh < 4; ++hh) {
    f16v p = mfma32(wpf[2 * hh], kf[hh], f16v{});
    p = mfma32(wpf[2 * hh + 1], kf[hh + 1], p);
    unsigned o[9];
    softmax2(p, o);
    LDS char* d = sm + (32 * w + r32) * PS + (2 * hh + h) * 40;
#pragma unroll
    for (int i = 0; i < 4; ++i) *(LDS u2*)(d + 8 * i) = u2{o[2 * i], o[2 * i + 1]};
    *(LDS u2*)(d + 32) = u2{o[8], 0u};
  }
  bar_lds();
  {
    const int hh = w >> 1, pi = 4 * ((l >> 4) & 1) + (l & 3), tq = 8 * h + ((l >> 2) & 3);
    LDS char* pa = sm + tq * PS + w * 40 + 8 * (pi < 4 ? pi : 4);
    LDS char* pb = sm + L_V + tq * VS + (pi < 5 ? 2 * (18 * hh - 2 * (hh & 1)) + 8 * pi : 144);
    f16v kv = {};
#pragma unroll
    for (int s = 0; s < 16; ++s) kv = mfma32(trr2(pa + s * 16 * PS, 4 * PS), trr2(pb + s * 16 * VS, 4 * VS), kv);
    if (r32 <= 20) {
#pragma unroll
      for (int a = 0; a < 3; ++a)
        __builtin_amdgcn_raw_buffer_store_b128(
            __builtin_bit_cast(u4, f4{kv[4 * a], kv[4 * a + 1], kv[4 * a + 2], kv[4 * a + 3]}), rws,
            KV_OFF + (((hg * 8 + m) * 8 + w) * 3 + a) * 1024 + l * 16, 0, 16);
    }
  }
  unsigned rq = 0;
  if (w == 0) rq = ld_flag(fl + (l & 7));
  u4 pq[4][3];
  auto phi_q = [&](int hh) {
    f16v p = mfma32(wpf[2 * hh], qf[hh], f16v{});
    p = mfma32(wpf[2 * hh + 1], qf[hh + 1], p);
    unsigned o[9];
    softmax2(p, o);
    pq[hh][0] = u4{o[0], o[1], o[2], o[3]}; pq[hh][1] = u4{o[4], o[5], o[6], o[7]}; pq[hh][2] = u4{o[8], 0u, 0u, 0u};
    asm volatile("" : "+v"(pq[hh][0]), "+v"(pq[hh][1]), "+v"(pq[hh][2]));
  };
  phi_q(0); phi_q(1);
  bar_all();
  if (w == 0 && l < 8) st_flag(fl + 8 + 8 * l + m, rq);
  phi_q(2); phi_q(3);
  if (w == 0) {
    for (int spin = 0; spin < (1 << 22); ++spin) {
      if (__builtin_amdgcn_ballot_w64(ld_flag(fl + 8 + 8 * m + (l & 7)) != nonce) == 0) break;
      rq = ld_flag(fl + (l & 7));
      if (l < 8) st_flag(fl + 8 + 8 * l + m, rq);
      __builtin_amdgcn_s_sleep(4);
    }
  }
  bar();
  if (w == 0) {
    rq = ld_flag(fl + (l & 7));
    if (l < 8) st_flag(fl + 8 + 8 * l + m, rq);
  }
  {
    const int hh = w >> 1, hd = w & 1;
    const int reg = (r32 & 3) + 4 * (r32 >> 3), c = reg < 9 ? 9 * ((r32 >> 2) & 1) + reg + 2 * (hh & 1) : 20;
    const unsigned o = KV_OFF + ((hg * 64 + 2 * hh + h) * 3) * 1024 + (c + 32 * hd) * 16;
    u4 t[3][8];
#pragma unroll
    for (int a = 0; a < 3; ++a)
#pragma unroll
      for (int mm = 0; mm < 8; ++mm)
        t[a][mm] = __builtin_amdgcn_raw_buffer_load_b128(rws, (a == 2 && hd) ? 0x80000000u : o, mm * 24576 + a * 1024, 16);
    SB();
#pragma unroll
    for (int a = 0; a < 3; ++a) {
      f4 s = {};
#pragma unroll
      for (int mm = 0; mm < 8; ++mm) s += __builtin_bit_cast(f4, t[a][mm]);
      *(LDS u2*)(sm + L_KVF + (hh * 3 + a) * 1024 + l * 16 + 8 * hd) = u2{pk2(s[0], s[1]), pk2(s[2], s[3])};
    }
  }
  bar_lds();
  float ov[36];
#pragma unroll
  for (int hh = 0; hh < 4; ++hh) {
    f16v o = {};
#pragma unroll
    for (int s = 0; s < 3; ++s) o = mfma32(*(LDS u4*)(sm + L_KVF + (hh * 3 + s) * 1024 + l * 16), pq[hh][s], o);
    const float rd = __builtin_amdgcn_rcpf(o[9] + 1e-6f);
#pragma unroll
    for (int i = 0; i < 9; ++i) ov[9 * hh + i] = o[i] * rd;
  }
  const unsigned ob = OP_OFF + ((16 * m + 2 * w + (r32 >> 4)) * 18 * 64 + (r32 & 15)) * 16;
#pragma unroll
  for (int i = 0; i < 4; ++i)
    __builtin_amdgcn_raw_buffer_store_b128(
        u4{pk2(ov[8 * i], ov[8 * i + 1]), pk2(ov[8 * i + 2], ov[8 * i + 3]), pk2(ov[8 * i + 4], ov[8 * i + 5]), pk2(ov[8 * i + 6], ov[8 * i + 7])},
        rws, ob + (9 * hg + 4 * h + i) * 256, 0, ST_AUX);
  __builtin_amdgcn_raw_buffer_store_b64(u2{pk2(ov[32], ov[33]), pk2(ov[34], ov[35])}, rws, ob + (9 * hg + 8) * 256 + 8 * h, 0, ST_AUX);
  }
}

__global__ void __launch_bounds__(512, 2)
k1(const char* __restrict__ x, const char* __restrict__ wqkv, const unsigned short* __restrict__ wphi,
   const char* __restrict__ wo, const char* __restrict__ wg, const char* __restrict__ wu,
   const char* __restrict__ wd, char* ws) {
  __shared__ __attribute__((aligned(1024))) char smem[163840];
  if ((blockIdx.x >> 5) < 6) k1_body<true>((LDS char*)smem, x, wqkv, wphi, wo, wg, wu, wd, ws);
  else k1_body<false>((LDS char*)smem, x, wqkv, wphi, wo, wg, wu, wd, ws);
}

__global__ void __launch_bounds__(512, 2)
k2(const char* __restrict__ x, char* ws, char* __restrict__ out) {
  __shared__ __attribute__((aligned(1024))) char smem[157696];
  LDS char* sm = (LDS char*)smem;
  const unsigned sb = (unsigned)(uintptr_t)sm;
  const int l = threadIdx.x & 63, w = __builtin_amdgcn_readfirstlane(threadIdx.x >> 6);
  const int xcd = blockIdx.x & 7, bi = blockIdx.x >> 3, q = xcd & 3;
  const int MT = 8 * (xcd >> 2) + (bi & 7), NT = bi >> 3, G = MT * 4 + NT;
  const rsrc_t rpx = __builtin_amdgcn_make_buffer_rsrc(ws + PX_OFF, 0, PX_SZ, 0x00020000);
  unsigned* fl = (unsigned*)(ws + F2_OFF) + G * 64;
  constexpr int SLOT = 17408, L_OWN = 8 * SLOT;
  const int lw = w & 3, kq = l >> 4, hf = w >> 2;
  const unsigned vl = l * 16, hl = (kq * 8 + (l & 7)) * 16;
  const unsigned xo = (128 * MT + 64 * (q >> 1) + 16 * lw + (l & 15)) * 1152 + (2 * NT + (q & 1)) * 144;
  const char* fH = ws + HP_OFF + ((8 * MT + lw) * 144 + 36 * q) * 1024;
  const char* fW = ws + WP_OFF + (2 * NT * 162 + 18 + 36 * q) * 4608;
  const char* sW = ws + WP_OFF + (2 * NT + (q & 1)) * (162 * 4608);
  const char* sO = ws + OP_OFF + (8 * MT + 4 * (q >> 1) + lw) * 18432;
  auto issue = [&](auto UU) {
    constexpr int U = decltype(UU)::v;
    const unsigned d = sb + (U % 8) * SLOT + lw * 1024;
    if constexpr (U < 36) {
      dma16(d, vl, fH + U * 1024);
      dma16(d + 8192, vl, fW + U * 4608 + lw * 1024);
      dma16(d + 4096, vl, fH + U * 1024 + 4 * 144 * 1024);
      dma16(d + 12288, vl, fW + (162 + U) * 4608 + lw * 1024);
      dma16_if<4 + (U & 1)>(w, sb + (U % 8) * SLOT + 16384, (l >> 5) * (162 * 4608) + 4096 + (l & 31) * 16, fW + U * 4608);
    } else if constexpr (U < 45) {
#pragma unroll
      for (int u = 0; u < 2; ++u) {
        dma16(d + u * 8192, vl, sO + (2 * (U - 36) + u) * 1024);
        dma16(d + u * 8192 + 4096, vl, sW + (2 * (U - 36) + u) * 4608 + lw * 1024);
      }
      dma16_if<4 + (U & 1)>(w, sb + (U % 8) * SLOT + 16384, (l >> 5) * 4608 + 4096 + (l & 31) * 16, sW + 2 * (U - 36) * 4608);
    }
  };
  if (hf) {
    sfor<0, 7>(issue);
    wait_vm<24>();
    bar();
    sfor<0, 18>([&](auto JJ) {
      constexpr int J = decltype(JJ)::v;
      if (w < 6) wait_vm<18>(); else wait_vm<16>();
      bar();
      issue(IC<2 * J + 7>{}); issue(IC<2 * J + 8>{});
    });
    wait_vm<0>();
    bar();
    issue(IC<43>{});
  } else {
    LDS char* rB = sm + (w >> 1) * 4096 + vl;
    LDS char* rA = sm + 8192 + (w & 1) * 4096 + vl;
    LDS char* rH = sm + 16384 + (w & 1) * 512 + hl;
    f4 acc[4][5];
    u4 A[2][5], B[2][4];
    auto rd = [&](auto UU, auto RR) {
      constexpr int U = decltype(UU)::v, r = decltype(RR)::v, o = (U % 8) * SLOT;
      if constexpr (U < 36) {
        if constexpr (r == 8) A[U % 2][4] = *(LDS u4*)(rH + o);
        else if constexpr (r & 1) A[U % 2][r / 2] = *(LDS u4*)(rA + o + (r / 2) * 1024);
        else B[U % 2][r / 2] = *(LDS u4*)(rB + o + (r / 2) * 1024);
      }
    };
    bar();
    sfor<0, 9>([&](auto RR) { rd(IC<0>{}, RR); });
    sfor<0, 36>([&](auto UU) {
      constexpr int U = decltype(UU)::v;
      if constexpr (U % 2 == 0) bar_lds();
      sfor<0, 20>([&](auto MM) {
        constexpr int n = decltype(MM)::v, i = n / 5, f = n % 5;
        acc[i][f] = mfma16(A[U % 2][f], B[U % 2][i], U == 0 ? f4{} : acc[i][f]);
        if constexpr (n % 2 == 0 && n < 18) rd(IC<U + 1>{}, IC<n / 2>{});
        SB();
      });
    });
    if (w == q) {
#pragma unroll
      for (int i = 0; i < 4; ++i) {
#pragma unroll
        for (int f = 0; f < 4; ++f) *(LDS f4*)(sm + L_OWN + i * 4608 + f * 1024 + vl) = acc[i][f];
        if (l < 32) *(LDS f4*)(sm + L_OWN + i * 4608 + 4096 + vl) = acc[i][4];
      }
    } else {
      const unsigned d = ((G * 4 + w) * 4 + q) * 9216;
#pragma unroll
      for (int i = 0; i < 4; ++i) {
#pragma unroll
        for (int P = 0; P < 2; ++P) {
          const f4 a0 = acc[i][2 * P], a1 = acc[i][2 * P + 1];
          __builtin_amdgcn_raw_buffer_store_b128(u4{pk2(a0[0], a0[1]), pk2(a0[2], a0[3]), pk2(a1[0], a1[1]), pk2(a1[2], a1[3])},
                                                 rpx, d + i * 2304 + P * 1024 + vl, 0, 16);
        }
        __builtin_amdgcn_raw_buffer_store_b64(u2{pk2(acc[i][4][0], acc[i][4][1]), pk2(acc[i][4][2], acc[i][4][3])}, rpx,
                                              l < 32 ? d + i * 2304 + 2048 + l * 8 : 0x80000000u, 0, 16);
      }
    }
    bar_lds();
  }
  f4 sa[3] = {};
  auto s_unit = [&](int U) {
    LDS char* b = sm + (U % 8) * SLOT;
#pragma unroll
    for (int u = 0; u < 2; ++u) {
      const u4 Bo = *(LDS u4*)(b + u * 8192 + lw * 1024 + vl);
      sa[0] = mfma16(*(LDS u4*)(b + u * 8192 + 4096 + hf * 2048 + vl), Bo, sa[0]);
      sa[1] = mfma16(*(LDS u4*)(b + u * 8192 + 5120 + hf * 2048 + vl), Bo, sa[1]);
      sa[2] = mfma16(*(LDS u4*)(b + 16384 + u * 512 + hl), Bo, sa[2]);
    }
  };
#pragma unroll
  for (int U = 36; U < 43; ++U) s_unit(U);
  bar_all();
  if (w == 0 && l < 3) st_flag(fl + ((q + 1 + l) & 3) * 16 + q, 1u);
  if (hf) issue(IC<44>{});
  const u4 xv = *(const u4*)(x + xo + 64 * hf + 16 * kq);
  const u2 xh = *(const u2*)(x + xo + 128 + 8 * (kq & 1));
  s_unit(43);
  if (w == 0) {
    for (int spin = 0; spin < (1 << 22); ++spin) {
      const unsigned e = ld_flag(fl + q * 16 + ((q + 1 + (l < 3 ? l : 0)) & 3));
      if (__builtin_amdgcn_ballot_w64(e != 1u) == 0) break;
      __builtin_amdgcn_s_sleep(2);
    }
  }
  bar_all();
  u4 r[3]; u2 rh[3];
#pragma unroll
  for (int j = 0; j < 3; ++j) {
    const unsigned so = ((G * 4 + q) * 4 + ((q + 1 + j) & 3)) * 9216 + lw * 2304;
    r[j] = __builtin_amdgcn_raw_buffer_load_b128(rpx, hf * 1024 + vl, so, 16);
    rh[j] = __builtin_amdgcn_raw_buffer_load_b64(rpx, hf ? 2048 + (l & 31) * 8 : 0x80000000u, so, 16);
  }
  const unsigned po = lw * 4608 + hf * 2048 + vl, ph = lw * 4608 + 4096 + (l & 31) * 16;
  f4 g[3] = {*(LDS f4*)(sm + L_OWN + po), *(LDS f4*)(sm + L_OWN + po + 1024), *(LDS f4*)(sm + L_OWN + ph)};
  s_unit(44);
#pragma unroll
  for (int j = 0; j < 3; ++j) {
    g[0] += f4{blo(r[j][0]), bhi(r[j][0]), blo(r[j][1]), bhi(r[j][1])};
    g[1] += f4{blo(r[j][2]), bhi(r[j][2]), blo(r[j][3]), bhi(r[j][3])};
    g[2] += f4{blo(rh[j][0]), bhi(rh[j][0]), blo(rh[j][1]), bhi(rh[j][1])};
  }
  *(u4*)(out + xo + 64 * hf + 16 * kq) =
      u4{pk2(blo(xv[0]) + silu_mul(g[0][0], sa[0][0]), bhi(xv[0]) + silu_mul(g[0][1], sa[0][1])),
         pk2(blo(xv[1]) + silu_mul(g[0][2], sa[0][2]), bhi(xv[1]) + silu_mul(g[0][3], sa[0][3])),
         pk2(blo(xv[2]) + silu_mul(g[1][0], sa[1][0]), bhi(xv[2]) + silu_mul(g[1][1], sa[1][1])),
         pk2(blo(xv[3]) + silu_mul(g[1][2], sa[1][2]), bhi(xv[3]) + silu_mul(g[1][3], sa[1][3]))};
  if (hf && kq < 2)
    *(u2*)(out + xo + 128 + 8 * kq) = u2{pk2(blo(xh[0]) + silu_mul(g[2][0], sa[2][0]), bhi(xh[0]) + silu_mul(g[2][1], sa[2][1])),
                                         pk2(blo(xh[1]) + silu_mul(g[2][2], sa[2][2]), bhi(xh[1]) + silu_mul(g[2][3], sa[2][3]))};
}

extern "C" void kernel_launch(void* const* d_in, const int* in_sizes, int n_in, void* d_out, int out_size,
                              void* d_ws, size_t ws_size, hipStream_t stream) {
  k1<<<256, 512, 0, stream>>>((const char*)d_in[0], (const char*)d_in[1], (const unsigned short*)d_in[2],
                              (const char*)d_in[3], (const char*)d_in[5], (const char*)d_in[6],
                              (const char*)d_in[7], (char*)d_ws);
  k2<<<256, 512, 0, stream>>>((const char*)d_in[0], (char*)d_ws, (char*)d_out);
}
```
